```python
import torch

def _softmax(x, axis=-1):
    return torch.softmax(x, dim=axis)
def _gelu(x):
    return torch.nn.functional.gelu(x, approximate="tanh")
def _silu(x):
    return torch.nn.functional.silu(x)
def _sigmoid(x):
    return torch.sigmoid(x)
def _layernorm(x, axis=-1, eps=1e-5):
    m = x.mean(dim=axis, keepdim=True)
    v = x.var(dim=axis, keepdim=True, unbiased=False)
    return (x - m) / torch.sqrt(v + eps)
def _scatter_add(tgt, idx, src, axis=0):
    return tgt.clone().index_add_(axis, idx, src)
def _segment_sum(src, seg, n):
    out = torch.zeros((n,) + src.shape[1:], dtype=src.dtype)
    return out.index_add_(0, seg, src)
def _pad(x, pw):
    flat = tuple(v for pair in reversed(pw) for v in pair)
    return torch.nn.functional.pad(x, flat)

kernel_name = 'recurrent_scan__sliding_block_cached__swiglu_ffn__decode'

def setup_inputs():

    _w = (lambda a: (lambda t: t.to(torch.bfloat16) if t.is_floating_point() else t)(torch.as_tensor(a)))
    def _A_setup():
        B, Sq, D = 8, 1, 512
        Hq, Hkv, Sk, Dh = 32, 8, 4096, 128
        assert Hq % Hkv == 0, (Hq, Hkv)
        return {
            "x": _w((lambda *s: torch.randn(*s, dtype=torch.bfloat16) * 0.05)(B, Sq, D)),
            "k_cache": _w((lambda *s: torch.randn(*s, dtype=torch.bfloat16) * 0.8)(B, Hkv, Sk, Dh)),
            "v_cache": _w((lambda *s: torch.randn(*s, dtype=torch.bfloat16) * 0.2)(B, Hkv, Sk, Dh)),
            "wq": _w((lambda *s: torch.randn(*s, dtype=torch.bfloat16) * 0.8)(D, Hq * Dh) * (D ** -0.5)),
            "wo": _w((lambda *s: torch.randn(*s, dtype=torch.bfloat16) * 0.2)(Hq * Dh, D) * ((Hq * Dh) ** -0.5)),
        }
    _A_in = _A_setup()

    def _B_setup():
        B, S, D, F = 8, 1, 512, 1792
        return {
            "x": _w((lambda *s: torch.randn(*s, dtype=torch.bfloat16) * 0.05)(B, S, D)),
            "w_gate": _w((lambda *s: torch.randn(*s, dtype=torch.bfloat16) * 0.1)(D, F)),
            "w_up": _w((lambda *s: torch.randn(*s, dtype=torch.bfloat16) * 0.1)(D, F)),
            "w_down": _w((lambda *s: torch.randn(*s, dtype=torch.bfloat16) * 0.1)(F, D)),
        }
    _B_in = _B_setup()

    return {
        "A_x": _A_in["x"],
        "A_k_cache": _A_in["k_cache"],
        "A_v_cache": _A_in["v_cache"],
        "A_wq": _A_in["wq"],
        "A_wo": _A_in["wo"],
        "B_x": _B_in["x"],
        "B_w_gate": _B_in["w_gate"],
        "B_w_up": _B_in["w_up"],
        "B_w_down": _B_in["w_down"],
    }

def reference(A_x, A_k_cache, A_v_cache, A_wq, A_wo, B_x, B_w_gate, B_w_up, B_w_down):
    def _A_ref(x, k_cache, v_cache, wq, wo):
        B, Sq, D = x.shape
        Hq, Dh = 32, 128
        Hkv, Sk = k_cache.shape[1], k_cache.shape[2]
        W = min(4096, max(256, Sk // 4))
        rep = Hq // Hkv
        q = (lambda x, *d: x.permute(*d))((x @ wq).reshape(B, Sq, Hq, Dh), 0, 2, 1, 3)
        k = (lambda x, n, axis: torch.repeat_interleave(x, n, dim=axis))(k_cache[:, :, Sk - W:, :], rep, axis=1)
        v = (lambda x, n, axis: torch.repeat_interleave(x, n, dim=axis))(v_cache[:, :, Sk - W:, :], rep, axis=1)
        scores = torch.einsum("bhqd,bhkd->bhqk", q, k) * (Dh ** -0.5)
        p = _softmax(scores, axis=-1)
        o = torch.einsum("bhqk,bhkd->bhqd", p, v)
        return (lambda x, *d: x.permute(*d))(o, 0, 2, 1, 3).reshape(B, Sq, Hq * Dh) @ wo

    def _B_ref(x, w_gate, w_up, w_down):
        g = _silu(x @ w_gate)
        u = x @ w_up
        return (g * u) @ w_down

    x = A_x
    xn = _layernorm(x, axis=-1)
    s = _A_ref(xn, A_k_cache, A_v_cache, A_wq, A_wo)
    g = _silu(_B_ref(xn, B_w_gate, B_w_up, B_w_down))
    return x + s * g
```

```cpp
#include <hip/hip_runtime.h>
#include <hip/hip_bf16.h>
#include <stdint.h>

typedef unsigned short u16;
typedef unsigned v4u __attribute__((ext_vector_type(4)));

constexpr int B = 8, D = 512, HQ = 32, HKV = 8, SK = 4096, DH = 128, W = 1024, F = 1792;
constexpr int NSPLIT = 4;

__device__ __forceinline__ float bf2f(u16 v) { return __uint_as_float(((unsigned)v) << 16); }
__device__ __forceinline__ float rbf(float f) { return __bfloat162float(__float2bfloat16(f)); }
__device__ __forceinline__ u16 f2bf(float f) { __hip_bfloat16 h = __float2bfloat16(f); return *(u16*)&h; }

__device__ void ln_all(const u16* x, float* xn  ) {
  int tid = threadIdx.x, lane = tid & 63, wave = tid >> 6, nw = blockDim.x >> 6;
  for (int r = wave; r < B; r += nw) {
    float v[8]; float s = 0;
    for (int j = 0; j < 8; ++j) { v[j] = bf2f(x[r * D + lane * 8 + j]); s += v[j]; }
    for (int o = 32; o; o >>= 1) s += __shfl_xor(s, o);
    float mean = s * (1.f / D);
    float q = 0;
    for (int j = 0; j < 8; ++j) { float d = v[j] - mean; q += d * d; }
    for (int o = 32; o; o >>= 1) q += __shfl_xor(q, o);
    float var = rbf(q * (1.f / D));
    float mb = rbf(mean);
    float den = rbf(sqrtf(rbf(var + 1e-5f)));
    for (int j = 0; j < 8; ++j) xn[r * D + lane * 8 + j] = rbf(rbf(v[j] - mb) / den);
  }
}

__global__ void __launch_bounds__(256) k1(const u16* x, const u16* wq, const u16* wg, const u16* wu, float* q, float* h) {
  __shared__ float xn[B * D];
  ln_all(x, xn);
  __syncthreads();
  int col = blockIdx.x * 256 + threadIdx.x;
  if (col < HQ * DH) {
    float acc[B] = {0};
    for (int k = 0; k < D; ++k) { float w = bf2f(wq[k * (HQ * DH) + col]);
      for (int b = 0; b < B; ++b) acc[b] += xn[b * D + k] * w; }
    for (int b = 0; b < B; ++b) q[b * HQ * DH + col] = rbf(acc[b]);
  } else {
    int c = col - HQ * DH;
    if (c < F) {
      float ag[B] = {0}, au[B] = {0};
      for (int k = 0; k < D; ++k) { float w1 = bf2f(wg[k * F + c]), w2 = bf2f(wu[k * F + c]);
        for (int b = 0; b < B; ++b) { ag[b] += xn[b * D + k] * w1; au[b] += xn[b * D + k] * w2; } }
      for (int b = 0; b < B; ++b) { float g = rbf(ag[b]); g = rbf(g / (1.f + __expf(-g))); h[b * F + c] = rbf(g * rbf(au[b])); }
    }
  }
}

__global__ void __launch_bounds__(256) k2(const float* q, const u16* kc, const u16* vc, float* o) {
  __shared__ float sc[W]; __shared__ float qs[DH]; __shared__ float red[8];
  int b = blockIdx.x / HQ, hq = blockIdx.x % HQ, kvh = hq / (HQ / HKV);
  int tid = threadIdx.x;
  if (tid < DH) qs[tid] = q[b * HQ * DH + hq * DH + tid];
  __syncthreads();
  const u16* kb = kc + ((size_t)(b * HKV + kvh) * SK + (SK - W)) * DH;
  const u16* vb = vc + ((size_t)(b * HKV + kvh) * SK + (SK - W)) * DH;
  float mx = -INFINITY;
  for (int j = tid; j < W; j += 256) { float s = 0; for (int d = 0; d < DH; ++d) s += qs[d] * bf2f(kb[j * DH + d]);
    s = rbf(rbf(s) * 0.08838834764831845f); sc[j] = s; mx = fmaxf(mx, s); }
  for (int of = 32; of; of >>= 1) mx = fmaxf(mx, __shfl_xor(mx, of));
  if ((tid & 63) == 0) red[tid >> 6] = mx;
  __syncthreads();
  mx = fmaxf(fmaxf(red[0], red[1]), fmaxf(red[2], red[3]));
  float sum = 0;
  for (int j = tid; j < W; j += 256) { float e = __expf(sc[j] - mx); sc[j] = e; sum += e; }
  for (int of = 32; of; of >>= 1) sum += __shfl_xor(sum, of);
  if ((tid & 63) == 0) red[4 + (tid >> 6)] = sum;
  __syncthreads();
  sum = red[4] + red[5] + red[6] + red[7];
  if (tid < DH) { float a = 0; for (int j = 0; j < W; ++j) a += rbf(sc[j] / sum) * bf2f(vb[j * DH + tid]);
    o[b * HQ * DH + hq * DH + tid] = rbf(a); }
}

__global__ void __launch_bounds__(512) k3(const u16* x, const float* o, const float* h, const u16* wo, const u16* wd, u16* out) {
  int b = blockIdx.x, c = threadIdx.x;
  float s = 0; for (int k = 0; k < HQ * DH; ++k) s += o[b * HQ * DH + k] * bf2f(wo[k * D + c]);
  float f = 0; for (int k = 0; k < F; ++k) f += h[b * F + k] * bf2f(wd[k * D + c]);
  s = rbf(s); f = rbf(f);
  float g = rbf(f / (1.f + __expf(-f)));
  out[b * D + c] = f2bf(bf2f(x[b * D + c]) + rbf(s * g));
}

extern "C" void kernel_launch(void* const* d_in, const int* in_sizes, int n_in, void* d_out, int out_size, void* d_ws, size_t ws_size, hipStream_t stream) {
  const u16* x = (const u16*)d_in[0]; const u16* kc = (const u16*)d_in[1]; const u16* vc = (const u16*)d_in[2];
  const u16* wq = (const u16*)d_in[3]; const u16* wo = (const u16*)d_in[4];
  const u16* wg = (const u16*)d_in[6]; const u16* wu = (const u16*)d_in[7]; const u16* wd = (const u16*)d_in[8];
  float* q = (float*)d_ws; float* h = q + B * HQ * DH; float* o = h + B * F;
  k1<<<(HQ * DH + F) / 256, 256, 0, stream>>>(x, wq, wg, wu, q, h);
  k2<<<B * HQ, 256, 0, stream>>>(q, kc, vc, o);
  k3<<<B, 512, 0, stream>>>(x, o, h, wo, wd, (u16*)d_out);
}
```

```cpp
#include <hip/hip_runtime.h>
#include <stdint.h>

#define AUX_STREAM 0
#define STAMPS 0

typedef unsigned short u16;
typedef unsigned long long u64;
typedef unsigned v4u __attribute__((ext_vector_type(4)));
typedef unsigned v2u __attribute__((ext_vector_type(2)));
typedef float f32x4 __attribute__((ext_vector_type(4)));
typedef float f32x2 __attribute__((ext_vector_type(2)));
typedef short bf16x8 __attribute__((ext_vector_type(8)));
typedef __amdgpu_buffer_rsrc_t rsrc_t;
#define LAS __attribute__((address_space(3)))
#define GAS __attribute__((address_space(1)))

constexpr int NB = 8, DM = 512, HQ = 32, HKV = 8, SK = 4096, DH = 128, WIN = 1024, FF = 1792;
constexpr int AUX_SC1 = 16;
constexpr int RSRC_W3 = 0x00020000;
constexpr unsigned SPIN_CAP = 1u << 18;

constexpr int WS_EPOCH = 0;
constexpr int WS_ML = 4096;
constexpr int WS_QP = 32768;
constexpr int WS_GU = WS_QP + (1 << 20);
constexpr int WS_OP = WS_GU + 56 * 16384;
constexpr int WS_SF = WS_OP + (1 << 20);
constexpr int WS_END = WS_SF + 256 * 15360;
constexpr int WS_STAMP = 8 << 20;

constexpr int L_K = 0;
constexpr int L_RED = 65536;
constexpr int L_XN = L_RED + 8 * 8 * 272;
constexpr int L_Q = L_XN + 8 * 288;
constexpr int L_H = L_Q + 4 * 288;
constexpr int L_M = L_H + 8 * 96;
constexpr int L_L = L_M + 128;
constexpr int L_O16 = L_L + 128;
constexpr int L_FIN = L_O16 + 8 * 160;
constexpr int L_END = L_FIN + 512;

__device__ __forceinline__ float bf_lo(unsigned u) { return __uint_as_float(u << 16); }
__device__ __forceinline__ float bf_hi(unsigned u) { return __uint_as_float(u & 0xffff0000u); }
__device__ __forceinline__ unsigned cvtpk(float lo, float hi) {
  typedef __bf16 bf16x2 __attribute__((ext_vector_type(2)));
  return __builtin_bit_cast(unsigned, __builtin_convertvector(f32x2{lo, hi}, bf16x2));
}
__device__ __forceinline__ float rbf(float f) { return bf_lo(cvtpk(f, f)); }
template <int CTRL> __device__ __forceinline__ float dpp(float x) {
  return __builtin_bit_cast(float, __builtin_amdgcn_mov_dpp(__builtin_bit_cast(int, x), CTRL, 0xf, 0xf, true));
}
constexpr int XOR1 = 0xB1, XOR2 = 0x4E, XOR7 = 0x141, XOR8 = 0x128;
__device__ __forceinline__ float xrow_sum(float x) {
  auto s = __builtin_amdgcn_permlane16_swap(__float_as_uint(x), __float_as_uint(x), false, false);
  x = __uint_as_float(s[0]) + __uint_as_float(s[1]);
  auto t = __builtin_amdgcn_permlane32_swap(__float_as_uint(x), __float_as_uint(x), false, false);
  return __uint_as_float(t[0]) + __uint_as_float(t[1]);
}
__device__ __forceinline__ float xrow_max(float x) {
  auto s = __builtin_amdgcn_permlane16_swap(__float_as_uint(x), __float_as_uint(x), false, false);
  x = fmaxf(__uint_as_float(s[0]), __uint_as_float(s[1]));
  auto t = __builtin_amdgcn_permlane32_swap(__float_as_uint(x), __float_as_uint(x), false, false);
  return fmaxf(__uint_as_float(t[0]), __uint_as_float(t[1]));
}
__device__ __forceinline__ float wave_sum(float x) {
  x += dpp<XOR1>(x); x += dpp<XOR2>(x); x += dpp<XOR7>(x); x += dpp<XOR8>(x);
  return xrow_sum(x);
}
__device__ __forceinline__ f32x4 mfma(v4u a, v4u b, f32x4 c) {
  return __builtin_amdgcn_mfma_f32_16x16x32_bf16(__builtin_bit_cast(bf16x8, a), __builtin_bit_cast(bf16x8, b), c, 0, 0, 0);
}
__device__ __forceinline__ unsigned plo(unsigned r1, unsigned r0) { return __builtin_amdgcn_perm(r1, r0, 0x05040100u); }
__device__ __forceinline__ unsigned phi(unsigned r1, unsigned r0) { return __builtin_amdgcn_perm(r1, r0, 0x07060302u); }
__device__ __forceinline__ rsrc_t mkrsrc(const void* p, unsigned bytes) { return __builtin_amdgcn_make_buffer_rsrc((void*)p, 0, bytes, RSRC_W3); }
__device__ __forceinline__ v4u unit(float a, float b, unsigned tag) { return v4u{__float_as_uint(a), tag, __float_as_uint(b), tag}; }
__device__ __forceinline__ bool uok(v4u u, unsigned tag) { return (u.y == tag) & (u.w == tag); }
__device__ __forceinline__ float silu(float x) { return x * __builtin_amdgcn_rcpf(1.f + __builtin_amdgcn_exp2f(-1.4426950408889634f * x)); }
#define SB() __builtin_amdgcn_sched_barrier(0)
#define POLL_SLEEP 1
__device__ __forceinline__ void repoll2(v4u& a, v4u& b, rsrc_t rs, int oa, int ob) {
  asm volatile("buffer_load_dwordx4 %0, %2, %4, 0 offen sc1\n\tbuffer_load_dwordx4 %1, %3, %4, 0 offen sc1\n\ts_waitcnt vmcnt(0)"
               : "+v"(a), "+v"(b) : "v"(oa), "v"(ob), "s"(rs) : "memory");
}
__device__ __forceinline__ void repoll3(v4u& a, v4u& b, v4u& c, rsrc_t rs, int oa, int oc) {
  asm volatile("buffer_load_dwordx4 %0, %3, %5, 0 offen sc1\n\tbuffer_load_dwordx4 %1, %3, %5, 0 offen offset:256 sc1\n\t"
               "buffer_load_dwordx4 %2, %4, %5, 0 offen sc1\n\ts_waitcnt vmcnt(0)"
               : "+v"(a), "+v"(b), "+v"(c) : "v"(oa), "v"(oc), "s"(rs) : "memory");
}
__device__ __forceinline__ void repoll4(v4u& a, v4u& b, v4u& c, v4u& d, rsrc_t rs, int oa, int ob, int oc, int od) {
  asm volatile("buffer_load_dwordx4 %0, %4, %8, 0 offen sc1\n\tbuffer_load_dwordx4 %1, %5, %8, 0 offen sc1\n\t"
               "buffer_load_dwordx4 %2, %6, %8, 0 offen sc1\n\tbuffer_load_dwordx4 %3, %7, %8, 0 offen sc1\n\ts_waitcnt vmcnt(0)"
               : "+v"(a), "+v"(b), "+v"(c), "+v"(d) : "v"(oa), "v"(ob), "v"(oc), "v"(od), "s"(rs) : "memory");
}
typedef short s16x4 __attribute__((ext_vector_type(4)));
__device__ __forceinline__ v2u trread(LAS unsigned char* p) { return __builtin_bit_cast(v2u, __builtin_amdgcn_ds_read_tr16_b64_v4i16((LAS s16x4*)p)); }
__device__ __forceinline__ int offb(int row, int ch) { return 256 * row + 16 * (ch ^ (((row & 3) << 2) | ((row >> 2) & 3))); }
#define BAR() do { asm volatile("s_waitcnt lgkmcnt(0)" ::: "memory"); __builtin_amdgcn_s_barrier(); asm volatile("" ::: "memory"); } while (0)
template <typename T> __device__ __forceinline__ T ldnt(const T* p) { return AUX_STREAM == 2 ? __builtin_nontemporal_load(p) : *p; }
#define STAMP(i)

__global__ void __launch_bounds__(512, 2)
fused_block(const u16* __restrict__ x, const u16* __restrict__ kc, const u16* __restrict__ vc, const u16* __restrict__ wq,
            const u16* __restrict__ wo, const u16* __restrict__ wg, const u16* __restrict__ wu, const u16* __restrict__ wd,
            u16* __restrict__ out, unsigned char* ws) {
  __shared__ __attribute__((aligned(16))) unsigned char lds_raw[L_END];
  LAS unsigned char* L = (LAS unsigned char*)lds_raw;
  STAMP(0);
  const int tid = threadIdx.x, lane = tid & 63, wv = __builtin_amdgcn_readfirstlane(tid >> 6);
  const int g = lane >> 4, c = lane & 15;
  const int w = blockIdx.x, xcd = w & 7, idx = w >> 3, part = idx & 3, grp = idx >> 2;
  const bool ffn = idx < 28;
  const int fr = xcd * 7 + grp;
  const int ri = xcd * 8 + grp;
  const rsrc_t rws = mkrsrc(ws, WS_END);
  const rsrc_t rx = mkrsrc(x, NB * DM * 2);
  const size_t kvoff = ((size_t)(grp * HKV + xcd) * SK + (SK - WIN) + part * 256) * DH;
  const rsrc_t rk = mkrsrc(kc + kvoff, 65536);
  const rsrc_t rv = mkrsrc(vc + kvoff, 65536);
  const bool front = (wv < 4) | ffn;

  const v4u xr = __builtin_amdgcn_raw_buffer_load_b128(rx, lane * 16, wv * 1024, 0);
  const unsigned ep = __builtin_amdgcn_raw_buffer_load_b32(rws, 0, WS_EPOCH, AUX_SC1);
  const unsigned xfin = __builtin_amdgcn_raw_buffer_load_b16(rx, (w * 16 + c) * 2, 0, 0);
  v2u fw[8];
  {
    const int krow = part * 128 + (wv & 3) * 32 + 8 * g;
    const u16* p = wv < 4 ? wq + (size_t)krow * (HQ * DH) + xcd * 512 + grp * 64 + 4 * c
                          : (ffn ? (c < 8 ? wg : wu) + (size_t)krow * FF + fr * 32 + (c & 7) * 4 : x + 4 * c);
    const int stride = wv < 4 ? HQ * DH : (ffn ? FF : 0);
#pragma unroll
    for (int j = 0; j < 8; ++j) fw[j] = ldnt((const v2u*)(p + j * stride));
  }
  SB();
  v4u kv[8];
#pragma unroll
  for (int t = 0; t < 8; ++t) kv[t] = __builtin_amdgcn_raw_buffer_load_b128(rk, wv * 8192 + lane * 16, t * 1024, AUX_STREAM);
  SB();

  {
    float v[8];
#pragma unroll
    for (int j = 0; j < 4; ++j) { v[2 * j] = bf_lo(xr[j]); v[2 * j + 1] = bf_hi(xr[j]); }
    float s = ((v[0] + v[1]) + (v[2] + v[3])) + ((v[4] + v[5]) + (v[6] + v[7]));
    const float mean = wave_sum(s) * (1.f / DM);
    float q = 0.f;
#pragma unroll
    for (int j = 0; j < 8; ++j) { const float d = v[j] - mean; q = fmaf(d, d, q); }
    const float var = rbf(wave_sum(q) * (1.f / DM));
    const float mb = rbf(mean);
    const float rden = __builtin_amdgcn_rcpf(rbf(__builtin_amdgcn_sqrtf(rbf(var + 1e-5f))));
    v4u o;
#pragma unroll
    for (int j = 0; j < 4; ++j) {
      const unsigned dpk = cvtpk(v[2 * j] - mb, v[2 * j + 1] - mb);
      o[j] = cvtpk(bf_lo(dpk) * rden, bf_hi(dpk) * rden);
    }
    if (g == part) *(LAS v4u*)(L + L_XN + wv * 288 + c * 16) = o;
  }
  STAMP(1);
  BAR();
  const unsigned tag = __builtin_amdgcn_readfirstlane(ep) + 1u;
  const bool up = c >= 8;

  {
    const v4u bx = *(LAS v4u*)(L + L_XN + (c & 7) * 288 + (wv & 3) * 64 + g * 16);
    f32x4 d[4];
#pragma unroll
    for (int dd = 0; dd < 4; ++dd) {
      v4u a;
#pragma unroll
      for (int jj = 0; jj < 4; ++jj) a[jj] = (dd & 1) ? phi(fw[2 * jj + 1][dd >> 1], fw[2 * jj][dd >> 1]) : plo(fw[2 * jj + 1][dd >> 1], fw[2 * jj][dd >> 1]);
      d[dd] = mfma(a, bx, f32x4{0.f, 0.f, 0.f, 0.f});
    }
#pragma unroll
    for (int rr = 0; rr < 2; ++rr) {
      f32x4 o;
#pragma unroll
      for (int dd = 0; dd < 4; ++dd) o[dd] = up ? d[dd][rr + 2] : d[dd][rr];
      *(LAS f32x4*)(L + L_RED + (wv * 8 + (c & 7)) * 272 + g * 64 + (rr + (up ? 2 : 0)) * 16) = o;
    }
  }
  STAMP(2);
  BAR();
  const rsrc_t rfr = mkrsrc(ws, front ? WS_END : 0);
  {
    const int t = tid & 255, b = t >> 5, pr = t & 31;
    const int ra = L_RED + ((wv & 4) * 8 + b) * 272 + (wv < 4 ? pr * 8 : pr * 4), rb = ra + (wv < 4 ? 4 : 128);
    float a0 = 0.f, a1 = 0.f;
#pragma unroll
    for (int k = 0; k < 4; ++k) { a0 += *(LAS float*)(L + ra + k * 8 * 272); a1 += *(LAS float*)(L + rb + k * 8 * 272); }
    const int off = wv < 4 ? WS_QP + ((xcd * 8 + b) * 4 + part) * 4096 + grp * 512 + pr * 16 : WS_GU + (fr * 4 + part) * 4096 + t * 16;
    __builtin_amdgcn_raw_buffer_store_b128(unit(a0, a1, tag), rfr, off, 0, AUX_SC1);
  }
  STAMP(3);
  SB();

  LAS unsigned char* Kw = L + L_K + wv * 8192;
#pragma unroll
  for (int t = 0; t < 4; ++t) *(LAS v4u*)(Kw + (4 * t + g) * 256 + ((c ^ (4 * t + g)) * 16)) = kv[t];
  SB();
  STAMP(4);
  const int goff = (wv < 4 ? WS_QP + (xcd * 8 + grp) * 16384 : WS_GU + fr * 16384) + (tid & 255) * 16;
  v4u pu[4];
#pragma unroll
  for (int k = 0; k < 4; ++k) pu[k] = __builtin_amdgcn_raw_buffer_load_b128(rfr, goff, k * 4096, AUX_SC1);
  SB();
  v4u vv[8];
#pragma unroll
  for (int t = 0; t < 4; ++t) vv[t] = __builtin_amdgcn_raw_buffer_load_b128(rv, wv * 8192 + g * 1024 + c * 16 + t * 256, 0, AUX_STREAM);
  SB();
#pragma unroll
  for (int t = 4; t < 8; ++t) *(LAS v4u*)(Kw + (4 * t + g) * 256 + ((c ^ ((4 * t + g) & 15)) * 16)) = kv[t];
  SB();
#pragma unroll
  for (int t = 4; t < 8; ++t) vv[t] = __builtin_amdgcn_raw_buffer_load_b128(rv, wv * 8192 + g * 1024 + c * 16 + (t - 4) * 256, 4096, AUX_STREAM);
  SB();
  STAMP(5);

  {
    bool bad = front & !(uok(pu[0], tag) & uok(pu[1], tag) & uok(pu[2], tag) & uok(pu[3], tag));
    if (__builtin_expect(__any(bad), 0)) {
      unsigned spin = 0;
      do {
        __builtin_amdgcn_s_sleep(POLL_SLEEP);
        if (bad) repoll4(pu[0], pu[1], pu[2], pu[3], rws, goff, goff + 4096, goff + 8192, goff + 12288);
        bad = front & !(uok(pu[0], tag) & uok(pu[1], tag) & uok(pu[2], tag) & uok(pu[3], tag));
      } while (__any(bad) && ++spin < SPIN_CAP);
    }
    const float a0 = (__uint_as_float(pu[0].x) + __uint_as_float(pu[1].x)) + (__uint_as_float(pu[2].x) + __uint_as_float(pu[3].x));
    const float a1 = (__uint_as_float(pu[0].z) + __uint_as_float(pu[1].z)) + (__uint_as_float(pu[2].z) + __uint_as_float(pu[3].z));
    const unsigned pk = cvtpk(a0, a1);
    if (wv < 4) {
      *(LAS unsigned*)(L + L_Q + wv * 288 + lane * 4) = pk;
    } else {
      const float sg = rbf(silu(bf_lo(pk)));
      const unsigned hb = cvtpk(sg * bf_hi(pk), 0.f);
      const int t = tid & 255;
      *(LAS u16*)(L + L_H + (t >> 5) * 96 + (t & 31) * 2) = (u16)hb;
    }
  }
  STAMP(6);
  BAR();

  constexpr float C2 = 0.08838834764831845f * 1.4426950408889634f;
  f32x4 s0 = {0.f, 0.f, 0.f, 0.f}, s1 = {0.f, 0.f, 0.f, 0.f};
#pragma unroll
  for (int i = 0; i < 4; ++i) {
    const v4u bq = *(LAS v4u*)(L + L_Q + (c >> 2) * 288 + i * 64 + g * 16);
    const v4u a0 = *(LAS v4u*)(Kw + c * 256 + (((4 * i + g) ^ c) * 16));
    const v4u a1 = *(LAS v4u*)(Kw + 4096 + c * 256 + (((4 * i + g) ^ c) * 16));
    s0 = mfma(a0, bq, s0);
    s1 = mfma(a1, bq, s1);
  }
  {
    float mx = fmaxf(fmaxf(fmaxf(s0[0], s0[1]), fmaxf(s0[2], s0[3])), fmaxf(fmaxf(s1[0], s1[1]), fmaxf(s1[2], s1[3])));
    mx = xrow_max(mx);
    if ((lane & 0x33) == 0) *(LAS float*)(L + L_M + (c >> 2) * 32 + wv * 4) = mx;
  }
  BAR();
  v4u pa;
  {
    const f32x4 m0 = *(LAS f32x4*)(L + L_M + (c >> 2) * 32), m1 = *(LAS f32x4*)(L + L_M + (c >> 2) * 32 + 16);
    const float M = fmaxf(fmaxf(fmaxf(m0[0], m0[1]), fmaxf(m0[2], m0[3])), fmaxf(fmaxf(m1[0], m1[1]), fmaxf(m1[2], m1[3])));
    const float mc = M * C2;
    float p[8];
#pragma unroll
    for (int r = 0; r < 4; ++r) { p[r] = __builtin_amdgcn_exp2f(fmaf(s0[r], C2, -mc)); p[4 + r] = __builtin_amdgcn_exp2f(fmaf(s1[r], C2, -mc)); }
    float ls = ((p[0] + p[1]) + (p[2] + p[3])) + ((p[4] + p[5]) + (p[6] + p[7]));
    ls = xrow_sum(ls);
    if ((lane & 0x33) == 0) *(LAS float*)(L + L_L + (c >> 2) * 32 + wv * 4) = ls;
#pragma unroll
    for (int j = 0; j < 4; ++j) pa[j] = cvtpk(p[2 * j], p[2 * j + 1]);
  }
  STAMP(7);
  SB();
  const rsrc_t rwo = mkrsrc(wo + (size_t)ri * 64 * DM + part * 128, 64 * DM * 2);
  const rsrc_t rwd = mkrsrc(wd + (size_t)(ffn ? fr : 0) * 32 * DM + part * 128, ffn ? 32 * DM * 2 : 0);
  v4u bw[3];
  bw[0] = __builtin_amdgcn_raw_buffer_load_b128(rwd, (4 * wv + g) * 1024 + c * 16, 0, AUX_STREAM);
  bw[1] = __builtin_amdgcn_raw_buffer_load_b128(rwo, (8 * wv + g) * 1024 + c * 16, 0, AUX_STREAM);
  bw[2] = __builtin_amdgcn_raw_buffer_load_b128(rwo, (8 * wv + g) * 1024 + c * 16, 4096, AUX_STREAM);
  SB();

  {
    float o[8];
#pragma unroll
    for (int dd = 0; dd < 8; ++dd) {
      v4u b;
#pragma unroll
      for (int jj = 0; jj < 4; ++jj) b[jj] = (dd & 1) ? phi(vv[2 * jj + 1][dd >> 1], vv[2 * jj][dd >> 1]) : plo(vv[2 * jj + 1][dd >> 1], vv[2 * jj][dd >> 1]);
      const f32x4 r = mfma(pa, b, f32x4{0.f, 0.f, 0.f, 0.f});
      o[dd] = r[0];
    }
    *(LAS f32x4*)(L + L_RED + wv * 2048 + g * 256 + c * 16) = f32x4{o[0], o[1], o[2], o[3]};
    *(LAS f32x4*)(L + L_RED + wv * 2048 + 1024 + g * 256 + c * 16) = f32x4{o[4], o[5], o[6], o[7]};
  }
  STAMP(8);
  BAR();
  {
    const int plane = lane >> 5, cc = (lane >> 1) & 15, lo = lane & 1;
    const int ra = wv < 4 ? L_RED + plane * 1024 + wv * 256 + cc * 16 + lo * 8 : L_M + (wv - 4) * 32;
    float a0, a1;
    if (wv < 4) {
      a0 = 0.f; a1 = 0.f;
#pragma unroll
      for (int k = 0; k < 8; ++k) { const f32x2 v = *(LAS f32x2*)(L + ra + k * 2048); a0 += v.x; a1 += v.y; }
    } else {
      const f32x4 m0 = *(LAS f32x4*)(L + ra), m1 = *(LAS f32x4*)(L + ra + 16);
      const f32x4 l0 = *(LAS f32x4*)(L + ra + 128), l1 = *(LAS f32x4*)(L + ra + 144);
      a0 = fmaxf(fmaxf(fmaxf(m0[0], m0[1]), fmaxf(m0[2], m0[3])), fmaxf(fmaxf(m1[0], m1[1]), fmaxf(m1[2], m1[3]))) * C2;
      a1 = ((l0[0] + l0[1]) + (l0[2] + l0[3])) + ((l1[0] + l1[1]) + (l1[2] + l1[3]));
    }
    const int off = wv < 4 ? WS_OP + ((((xcd * 4 + wv) * 2 + (cc >> 3)) * 8 + grp) * 4 + part) * 512 + ((cc & 7) * 8 + plane * 4 + lo * 2) * 8
                           : (lane == 0 ? WS_ML + (((xcd * 4 + wv - 4) * 8 + grp) * 4 + part) * 16 : WS_END);
    __builtin_amdgcn_raw_buffer_store_b128(unit(a0, a1, tag), rws, off, 0, AUX_SC1);
  }
  STAMP(9);
  SB();

  const int obase = WS_OP + ((ri * 8 + wv) * 4 + g) * 512 + c * 16;
  const int mlbase = WS_ML + (((ri >> 1) * 8 + wv) * 4 + g) * 16;
  v4u ou0 = __builtin_amdgcn_raw_buffer_load_b128(rws, obase, 0, AUX_SC1);
  v4u ou1 = __builtin_amdgcn_raw_buffer_load_b128(rws, obase + 256, 0, AUX_SC1);
  v4u mlu = __builtin_amdgcn_raw_buffer_load_b128(rws, mlbase, 0, AUX_SC1);
  SB();
  *(LAS v4u*)(L + L_K + offb(64 + 4 * wv + g, c)) = bw[0];
  *(LAS v4u*)(L + L_K + offb(8 * wv + g, c)) = bw[1];
  *(LAS v4u*)(L + L_K + offb(8 * wv + 4 + g, c)) = bw[2];
  STAMP(10);
  BAR();
  v4u af[3];
#pragma unroll
  for (int ks = 0; ks < 3; ++ks) {
    const v2u lo = trread(L + L_K + offb(32 * ks + 8 * g + (c >> 2), 2 * wv + ((c & 3) >> 1)) + 8 * (c & 1));
    const v2u hi = trread(L + L_K + offb(32 * ks + 8 * g + 4 + (c >> 2), 2 * wv + ((c & 3) >> 1)) + 8 * (c & 1));
    af[ks] = v4u{lo.x, lo.y, hi.x, hi.y};
  }
  const int sfoff = WS_SF + ((c & 7) * 32 + part * 8 + wv) * 15360 + (2 * g + (up ? 1 : 0)) * 16;
  {
    const v4u bh = *(LAS v4u*)(L + L_H + (c & 7) * 96 + g * 16);
    const f32x4 d = mfma(af[2], bh, f32x4{0.f, 0.f, 0.f, 0.f});
    __builtin_amdgcn_raw_buffer_store_b128(unit(up ? d[2] : d[0], up ? d[3] : d[1], tag), rws, ffn ? sfoff + (64 + fr) * 128 : WS_END, 0, AUX_SC1);
  }
  SB();
  {
    bool bad = !(uok(ou0, tag) & uok(ou1, tag) & uok(mlu, tag));
    if (__builtin_expect(__any(bad), 0)) {
      unsigned spin = 0;
      do {
        __builtin_amdgcn_s_sleep(POLL_SLEEP);
        if (bad) repoll3(ou0, ou1, mlu, rws, obase, mlbase);
        bad = !(uok(ou0, tag) & uok(ou1, tag) & uok(mlu, tag));
      } while (__any(bad) && ++spin < SPIN_CAP);
    }
  }
  STAMP(11);
  {
    const float ms = __uint_as_float(mlu.x), lsp = __uint_as_float(mlu.z);
    const float M = xrow_max(ms);
    const float e = __builtin_amdgcn_exp2f(ms - M);
    const float Lt = xrow_sum(e * lsp);
    const float wgt = e * __builtin_amdgcn_rcpf(Lt);
    const float o0 = xrow_sum(wgt * __uint_as_float(ou0.x)), o1 = xrow_sum(wgt * __uint_as_float(ou0.z));
    const float o2 = xrow_sum(wgt * __uint_as_float(ou1.x)), o3 = xrow_sum(wgt * __uint_as_float(ou1.z));
    const unsigned pk = cvtpk(g == 0 ? o0 : o2, g == 0 ? o1 : o3);
    if (g < 2) *(LAS unsigned*)(L + L_O16 + wv * 160 + g * 64 + c * 4) = pk;
  }
  BAR();
  {
    const v4u bo0 = *(LAS v4u*)(L + L_O16 + (c & 7) * 160 + g * 16), bo1 = *(LAS v4u*)(L + L_O16 + (c & 7) * 160 + 64 + g * 16);
    f32x4 d = mfma(af[0], bo0, f32x4{0.f, 0.f, 0.f, 0.f});
    d = mfma(af[1], bo1, d);
    __builtin_amdgcn_raw_buffer_store_b128(unit(up ? d[2] : d[0], up ? d[3] : d[1], tag), rws, sfoff + ri * 128, 0, AUX_SC1);
  }
  STAMP(12);
  SB();

  {
    const int line0 = wv < 4 ? 16 * wv : 64 + 14 * (wv - 4);
    const int fa = WS_SF + w * 15360 + line0 * 128 + lane * 16;
    const bool two = (wv < 4) | (lane < 48);
    const int fb = two ? fa + 1024 : fa;
    v4u f0 = __builtin_amdgcn_raw_buffer_load_b128(rws, fa, 0, AUX_SC1);
    v4u f1 = __builtin_amdgcn_raw_buffer_load_b128(rws, fb, 0, AUX_SC1);
    bool bad = !(uok(f0, tag) & uok(f1, tag));
    if (__any(bad)) {
      unsigned spin = 0;
      do {
        __builtin_amdgcn_s_sleep(POLL_SLEEP);
        if (bad) repoll2(f0, f1, rws, fa, fb);
        bad = !(uok(f0, tag) & uok(f1, tag));
      } while (__any(bad) && ++spin < SPIN_CAP);
    }
    STAMP(13);
    float a0 = __uint_as_float(f0.x) + (two ? __uint_as_float(f1.x) : 0.f);
    float a1 = __uint_as_float(f0.z) + (two ? __uint_as_float(f1.z) : 0.f);
    a0 += dpp<XOR8>(a0); a1 += dpp<XOR8>(a1);
    a0 = xrow_sum(a0); a1 = xrow_sum(a1);
    if (lane < 8) *(LAS f32x2*)(L + L_FIN + wv * 64 + lane * 8) = f32x2{a0, a1};
  }
  BAR();
  if (wv == 0 && lane < 16) {
    float s = 0.f, f = 0.f;
#pragma unroll
    for (int k = 0; k < 4; ++k) { s += *(LAS float*)(L + L_FIN + k * 64 + lane * 4); f += *(LAS float*)(L + L_FIN + (4 + k) * 64 + lane * 4); }
    const float gt = rbf(silu(rbf(f)));
    const float y = bf_lo(xfin) + rbf(rbf(s) * gt);
    out[w * 16 + lane] = (u16)cvtpk(y, y);
    if (w == 0 && lane == 0) __builtin_amdgcn_raw_buffer_store_b32(tag, rws, 0, WS_EPOCH, AUX_SC1);
  }
}

extern "C" void kernel_launch(void* const* d_in, const int* in_sizes, int n_in, void* d_out, int out_size, void* d_ws, size_t ws_size,
                              hipStream_t stream) {
  fused_block<<<256, 512, 0, stream>>>((const u16*)d_in[0], (const u16*)d_in[1], (const u16*)d_in[2], (const u16*)d_in[3], (const u16*)d_in[4],
                                       (const u16*)d_in[6], (const u16*)d_in[7], (const u16*)d_in[8], (u16*)d_out, (unsigned char*)d_ws);
}
```

```cpp
#define STAMPS 1
#define LEAK 0
#include <hip/hip_runtime.h>
#include <stdint.h>

#define AUX_STREAM 2

typedef unsigned short u16;
typedef unsigned long long u64;
typedef unsigned v4u __attribute__((ext_vector_type(4)));
typedef unsigned v2u __attribute__((ext_vector_type(2)));
typedef float f32x4 __attribute__((ext_vector_type(4)));
typedef float f32x2 __attribute__((ext_vector_type(2)));
typedef short bf16x8 __attribute__((ext_vector_type(8)));
typedef __amdgpu_buffer_rsrc_t rsrc_t;
#define LAS __attribute__((address_space(3)))
#define GAS __attribute__((address_space(1)))

constexpr int NB = 8, DM = 512, HQ = 32, HKV = 8, SK = 4096, DH = 128, WIN = 1024, FF = 1792;
constexpr int AUX_SC1 = 16;
constexpr int RSRC_W3 = 0x00020000;
constexpr unsigned SPIN_CAP = 1u << 18;

constexpr int WS_EPOCH = 0;
constexpr int WS_ML = 4096;
constexpr int WS_QP = 32768;
constexpr int WS_GU = WS_QP + (1 << 20);
constexpr int WS_OP = WS_GU + 56 * 16384;
constexpr int WS_SF = WS_OP + (1 << 20);
constexpr int WS_END = WS_SF + 256 * 15360;
constexpr int WS_STAMP = 8 << 20;

constexpr int L_K = 0;
constexpr int L_RED = 65536;
constexpr int L_XN = L_RED + 8 * 8 * 272;
constexpr int L_Q = L_XN + 8 * 288;
constexpr int L_H = L_Q + 4 * 288;
constexpr int L_M = L_H + 8 * 96;
constexpr int L_L = L_M + 128;
constexpr int L_O16 = L_L + 128;
constexpr int L_FIN = L_O16 + 8 * 160;
constexpr int L_END = L_FIN + 512;

__device__ __forceinline__ float bf_lo(unsigned u) { return __uint_as_float(u << 16); }
__device__ __forceinline__ float bf_hi(unsigned u) { return __uint_as_float(u & 0xffff0000u); }
__device__ __forceinline__ unsigned cvtpk(float lo, float hi) {
  typedef __bf16 bf16x2 __attribute__((ext_vector_type(2)));
  return __builtin_bit_cast(unsigned, __builtin_convertvector(f32x2{lo, hi}, bf16x2));
}
__device__ __forceinline__ float rbf(float f) { return bf_lo(cvtpk(f, f)); }
template <int CTRL> __device__ __forceinline__ float dpp(float x) {
  return __builtin_bit_cast(float, __builtin_amdgcn_mov_dpp(__builtin_bit_cast(int, x), CTRL, 0xf, 0xf, true));
}
constexpr int XOR1 = 0xB1, XOR2 = 0x4E, XOR7 = 0x141, XOR8 = 0x128;
__device__ __forceinline__ float xrow_sum(float x) {
  auto s = __builtin_amdgcn_permlane16_swap(__float_as_uint(x), __float_as_uint(x), false, false);
  x = __uint_as_float(s[0]) + __uint_as_float(s[1]);
  auto t = __builtin_amdgcn_permlane32_swap(__float_as_uint(x), __float_as_uint(x), false, false);
  return __uint_as_float(t[0]) + __uint_as_float(t[1]);
}
__device__ __forceinline__ float xrow_max(float x) {
  auto s = __builtin_amdgcn_permlane16_swap(__float_as_uint(x), __float_as_uint(x), false, false);
  x = fmaxf(__uint_as_float(s[0]), __uint_as_float(s[1]));
  auto t = __builtin_amdgcn_permlane32_swap(__float_as_uint(x), __float_as_uint(x), false, false);
  return fmaxf(__uint_as_float(t[0]), __uint_as_float(t[1]));
}
__device__ __forceinline__ float wave_sum(float x) {
  x += dpp<XOR1>(x); x += dpp<XOR2>(x); x += dpp<XOR7>(x); x += dpp<XOR8>(x);
  return xrow_sum(x);
}
__device__ __forceinline__ f32x4 mfma(v4u a, v4u b, f32x4 c) {
  return __builtin_amdgcn_mfma_f32_16x16x32_bf16(__builtin_bit_cast(bf16x8, a), __builtin_bit_cast(bf16x8, b), c, 0, 0, 0);
}
__device__ __forceinline__ unsigned plo(unsigned r1, unsigned r0) { return __builtin_amdgcn_perm(r1, r0, 0x05040100u); }
__device__ __forceinline__ unsigned phi(unsigned r1, unsigned r0) { return __builtin_amdgcn_perm(r1, r0, 0x07060302u); }
__device__ __forceinline__ rsrc_t mkrsrc(const void* p, unsigned bytes) { return __builtin_amdgcn_make_buffer_rsrc((void*)p, 0, bytes, RSRC_W3); }
__device__ __forceinline__ v4u unit(float a, float b, unsigned tag) { return v4u{__float_as_uint(a), tag, __float_as_uint(b), tag}; }
__device__ __forceinline__ bool uok(v4u u, unsigned tag) { return (u.y == tag) & (u.w == tag); }
__device__ __forceinline__ float silu(float x) { return x * __builtin_amdgcn_rcpf(1.f + __builtin_amdgcn_exp2f(-1.4426950408889634f * x)); }
#define SB() __builtin_amdgcn_sched_barrier(0)
#define POLL_SLEEP 1
__device__ __forceinline__ void repoll2(v4u& a, v4u& b, rsrc_t rs, int oa, int ob) {
  asm volatile("buffer_load_dwordx4 %0, %2, %4, 0 offen sc1\n\tbuffer_load_dwordx4 %1, %3, %4, 0 offen sc1\n\ts_waitcnt vmcnt(0)"
               : "+v"(a), "+v"(b) : "v"(oa), "v"(ob), "s"(rs) : "memory");
}
__device__ __forceinline__ void repoll3(v4u& a, v4u& b, v4u& c, rsrc_t rs, int oa, int oc) {
  asm volatile("buffer_load_dwordx4 %0, %3, %5, 0 offen sc1\n\tbuffer_load_dwordx4 %1, %3, %5, 0 offen offset:256 sc1\n\t"
               "buffer_load_dwordx4 %2, %4, %5, 0 offen sc1\n\ts_waitcnt vmcnt(0)"
               : "+v"(a), "+v"(b), "+v"(c) : "v"(oa), "v"(oc), "s"(rs) : "memory");
}
__device__ __forceinline__ void repoll4(v4u& a, v4u& b, v4u& c, v4u& d, rsrc_t rs, int oa, int ob, int oc, int od) {
  asm volatile("buffer_load_dwordx4 %0, %4, %8, 0 offen sc1\n\tbuffer_load_dwordx4 %1, %5, %8, 0 offen sc1\n\t"
               "buffer_load_dwordx4 %2, %6, %8, 0 offen sc1\n\tbuffer_load_dwordx4 %3, %7, %8, 0 offen sc1\n\ts_waitcnt vmcnt(0)"
               : "+v"(a), "+v"(b), "+v"(c), "+v"(d) : "v"(oa), "v"(ob), "v"(oc), "v"(od), "s"(rs) : "memory");
}
typedef short s16x4 __attribute__((ext_vector_type(4)));
__device__ __forceinline__ v2u trread(LAS unsigned char* p) { return __builtin_bit_cast(v2u, __builtin_amdgcn_ds_read_tr16_b64_v4i16((LAS s16x4*)p)); }
__device__ __forceinline__ int offb(int row, int ch) { return 256 * row + 16 * (ch ^ (((row & 3) << 2) | ((row >> 2) & 3))); }
#define BAR() do { asm volatile("s_waitcnt lgkmcnt(0)" ::: "memory"); __builtin_amdgcn_s_barrier(); asm volatile("" ::: "memory"); } while (0)
template <typename T> __device__ __forceinline__ T ldnt(const T* p) { return AUX_STREAM == 2 ? __builtin_nontemporal_load(p) : *p; }
#define STAMP(i) do { SB(); st[i] = __builtin_amdgcn_s_memrealtime(); SB(); } while (0)

__global__ void __launch_bounds__(512, 2)
fused_block(const u16* __restrict__ x, const u16* __restrict__ kc, const u16* __restrict__ vc, const u16* __restrict__ wq,
            const u16* __restrict__ wo, const u16* __restrict__ wg, const u16* __restrict__ wu, const u16* __restrict__ wd,
            u16* __restrict__ out, unsigned char* ws) {
  __shared__ __attribute__((aligned(16))) unsigned char lds_raw[L_END];
  LAS unsigned char* L = (LAS unsigned char*)lds_raw;
  u64 st[16];
#pragma unroll
  for (int i = 0; i < 16; ++i) st[i] = 0;
  unsigned nsp0 = 0, nsp1 = 0, nsp2 = 0;
  STAMP(0);
  const int tid = threadIdx.x, lane = tid & 63, wv = __builtin_amdgcn_readfirstlane(tid >> 6);
  const int g = lane >> 4, c = lane & 15;
  const int w = blockIdx.x, xcd = w & 7, idx = w >> 3, part = idx & 3, grp = idx >> 2;
  const bool ffn = idx < 28;
  const int fr = xcd * 7 + grp;
  const int ri = xcd * 8 + grp;
  const rsrc_t rws = mkrsrc(ws, WS_END);
  const rsrc_t rx = mkrsrc(x, NB * DM * 2);
  const size_t kvoff = ((size_t)(grp * HKV + xcd) * SK + (SK - WIN) + part * 256) * DH;
  const rsrc_t rk = mkrsrc(kc + kvoff, 65536);
  const rsrc_t rv = mkrsrc(vc + kvoff, 65536);
  const bool front = (wv < 4) | ffn;

  const v4u xr = __builtin_amdgcn_raw_buffer_load_b128(rx, lane * 16, wv * 1024, 0);
  const unsigned ep = __builtin_amdgcn_raw_buffer_load_b32(rws, 0, WS_EPOCH, AUX_SC1);
  const unsigned xfin = __builtin_amdgcn_raw_buffer_load_b16(rx, (w * 16 + c) * 2, 0, 0);
  v2u fw[8];
  {
    const int krow = part * 128 + (wv & 3) * 32 + 8 * g;
    const u16* p = wv < 4 ? wq + (size_t)krow * (HQ * DH) + xcd * 512 + grp * 64 + 4 * c
                          : (ffn ? (c < 8 ? wg : wu) + (size_t)krow * FF + fr * 32 + (c & 7) * 4 : x + 4 * c);
    const int stride = wv < 4 ? HQ * DH : (ffn ? FF : 0);
#pragma unroll
    for (int j = 0; j < 8; ++j) fw[j] = ldnt((const v2u*)(p + j * stride));
  }
  SB();
  v4u kv[8];
#pragma unroll
  for (int t = 0; t < 8; ++t) kv[t] = __builtin_amdgcn_raw_buffer_load_b128(rk, wv * 8192 + lane * 16, t * 1024, AUX_STREAM);
  SB();

  {
    float v[8];
#pragma unroll
    for (int j = 0; j < 4; ++j) { v[2 * j] = bf_lo(xr[j]); v[2 * j + 1] = bf_hi(xr[j]); }
    float s = ((v[0] + v[1]) + (v[2] + v[3])) + ((v[4] + v[5]) + (v[6] + v[7]));
    const float mean = wave_sum(s) * (1.f / DM);
    float q = 0.f;
#pragma unroll
    for (int j = 0; j < 8; ++j) { const float d = v[j] - mean; q = fmaf(d, d, q); }
    const float var = rbf(wave_sum(q) * (1.f / DM));
    const float mb = rbf(mean);
    const float rden = __builtin_amdgcn_rcpf(rbf(__builtin_amdgcn_sqrtf(rbf(var + 1e-5f))));
    v4u o;
#pragma unroll
    for (int j = 0; j < 4; ++j) {
      const unsigned dpk = cvtpk(v[2 * j] - mb, v[2 * j + 1] - mb);
      o[j] = cvtpk(bf_lo(dpk) * rden, bf_hi(dpk) * rden);
    }
    if (g == part) *(LAS v4u*)(L + L_XN + wv * 288 + c * 16) = o;
  }
  STAMP(1);
  BAR();
  const unsigned tag = __builtin_amdgcn_readfirstlane(ep) + 1u;
  const bool up = c >= 8;

  {
    const v4u bx = *(LAS v4u*)(L + L_XN + (c & 7) * 288 + (wv & 3) * 64 + g * 16);
    f32x4 d[4];
#pragma unroll
    for (int dd = 0; dd < 4; ++dd) {
      v4u a;
#pragma unroll
      for (int jj = 0; jj < 4; ++jj) a[jj] = (dd & 1) ? phi(fw[2 * jj + 1][dd >> 1], fw[2 * jj][dd >> 1]) : plo(fw[2 * jj + 1][dd >> 1], fw[2 * jj][dd >> 1]);
      d[dd] = mfma(a, bx, f32x4{0.f, 0.f, 0.f, 0.f});
    }
#pragma unroll
    for (int rr = 0; rr < 2; ++rr) {
      f32x4 o;
#pragma unroll
      for (int dd = 0; dd < 4; ++dd) o[dd] = up ? d[dd][rr + 2] : d[dd][rr];
      *(LAS f32x4*)(L + L_RED + (wv * 8 + (c & 7)) * 272 + g * 64 + (rr + (up ? 2 : 0)) * 16) = o;
    }
  }
  STAMP(2);
  BAR();
  const rsrc_t rfr = mkrsrc(ws, front ? WS_END : 0);
  {
    const int t = tid & 255, b = t >> 5, pr = t & 31;
    const int ra = L_RED + ((wv & 4) * 8 + b) * 272 + (wv < 4 ? pr * 8 : pr * 4), rb = ra + (wv < 4 ? 4 : 128);
    float a0 = 0.f, a1 = 0.f;
#pragma unroll
    for (int k = 0; k < 4; ++k) { a0 += *(LAS float*)(L + ra + k * 8 * 272); a1 += *(LAS float*)(L + rb + k * 8 * 272); }
    const int off = wv < 4 ? WS_QP + ((xcd * 8 + b) * 4 + part) * 4096 + grp * 512 + pr * 16 : WS_GU + (fr * 4 + part) * 4096 + t * 16;
    __builtin_amdgcn_raw_buffer_store_b128(unit(a0, a1, tag), rfr, off, 0, AUX_SC1);
  }
  STAMP(3);
  SB();

  LAS unsigned char* Kw = L + L_K + wv * 8192;
#pragma unroll
  for (int t = 0; t < 4; ++t) *(LAS v4u*)(Kw + (4 * t + g) * 256 + ((c ^ (4 * t + g)) * 16)) = kv[t];
  SB();
  STAMP(4);
  const int goff = (wv < 4 ? WS_QP + (xcd * 8 + grp) * 16384 : WS_GU + fr * 16384) + (tid & 255) * 16;
  v4u pu[4];
#pragma unroll
  for (int k = 0; k < 4; ++k) pu[k] = __builtin_amdgcn_raw_buffer_load_b128(rfr, goff, k * 4096, AUX_SC1);
  SB();
  v4u vv[8];
#pragma unroll
  for (int t = 0; t < 4; ++t) vv[t] = __builtin_amdgcn_raw_buffer_load_b128(rv, wv * 8192 + g * 1024 + c * 16 + t * 256, 0, AUX_STREAM);
  SB();
#pragma unroll
  for (int t = 4; t < 8; ++t) *(LAS v4u*)(Kw + (4 * t + g) * 256 + ((c ^ ((4 * t + g) & 15)) * 16)) = kv[t];
  SB();
#pragma unroll
  for (int t = 4; t < 8; ++t) vv[t] = __builtin_amdgcn_raw_buffer_load_b128(rv, wv * 8192 + g * 1024 + c * 16 + (t - 4) * 256, 4096, AUX_STREAM);
  SB();
  STAMP(5);

  {
    bool bad = front & !(uok(pu[0], tag) & uok(pu[1], tag) & uok(pu[2], tag) & uok(pu[3], tag));
    if (__builtin_expect(__any(bad), 0)) {
      unsigned spin = 0;
      do {
        __builtin_amdgcn_s_sleep(POLL_SLEEP);
        if (bad) repoll4(pu[0], pu[1], pu[2], pu[3], rws, goff, goff + 4096, goff + 8192, goff + 12288);
        bad = front & !(uok(pu[0], tag) & uok(pu[1], tag) & uok(pu[2], tag) & uok(pu[3], tag));
      } while (__any(bad) && ++spin < SPIN_CAP);
      nsp0 = spin + 1;
    }
    const float a0 = (__uint_as_float(pu[0].x) + __uint_as_float(pu[1].x)) + (__uint_as_float(pu[2].x) + __uint_as_float(pu[3].x));
    const float a1 = (__uint_as_float(pu[0].z) + __uint_as_float(pu[1].z)) + (__uint_as_float(pu[2].z) + __uint_as_float(pu[3].z));
    const unsigned pk = cvtpk(a0, a1);
    if (wv < 4) {
      *(LAS unsigned*)(L + L_Q + wv * 288 + lane * 4) = pk;
    } else {
      const float sg = rbf(silu(bf_lo(pk)));
      const unsigned hb = cvtpk(sg * bf_hi(pk), 0.f);
      const int t = tid & 255;
      *(LAS u16*)(L + L_H + (t >> 5) * 96 + (t & 31) * 2) = (u16)hb;
    }
  }
  STAMP(6);
  BAR();

  constexpr float C2 = 0.08838834764831845f * 1.4426950408889634f;
  f32x4 s0 = {0.f, 0.f, 0.f, 0.f}, s1 = {0.f, 0.f, 0.f, 0.f};
#pragma unroll
  for (int i = 0; i < 4; ++i) {
    const v4u bq = *(LAS v4u*)(L + L_Q + (c >> 2) * 288 + i * 64 + g * 16);
    const v4u a0 = *(LAS v4u*)(Kw + c * 256 + (((4 * i + g) ^ c) * 16));
    const v4u a1 = *(LAS v4u*)(Kw + 4096 + c * 256 + (((4 * i + g) ^ c) * 16));
    s0 = mfma(a0, bq, s0);
    s1 = mfma(a1, bq, s1);
  }
  {
    float mx = fmaxf(fmaxf(fmaxf(s0[0], s0[1]), fmaxf(s0[2], s0[3])), fmaxf(fmaxf(s1[0], s1[1]), fmaxf(s1[2], s1[3])));
    mx = xrow_max(mx);
    if ((lane & 0x33) == 0) *(LAS float*)(L + L_M + (c >> 2) * 32 + wv * 4) = mx;
  }
  BAR();
  v4u pa;
  {
    const f32x4 m0 = *(LAS f32x4*)(L + L_M + (c >> 2) * 32), m1 = *(LAS f32x4*)(L + L_M + (c >> 2) * 32 + 16);
    const float M = fmaxf(fmaxf(fmaxf(m0[0], m0[1]), fmaxf(m0[2], m0[3])), fmaxf(fmaxf(m1[0], m1[1]), fmaxf(m1[2], m1[3])));
    const float mc = M * C2;
    float p[8];
#pragma unroll
    for (int r = 0; r < 4; ++r) { p[r] = __builtin_amdgcn_exp2f(fmaf(s0[r], C2, -mc)); p[4 + r] = __builtin_amdgcn_exp2f(fmaf(s1[r], C2, -mc)); }
    float ls = ((p[0] + p[1]) + (p[2] + p[3])) + ((p[4] + p[5]) + (p[6] + p[7]));
    ls = xrow_sum(ls);
    if ((lane & 0x33) == 0) *(LAS float*)(L + L_L + (c >> 2) * 32 + wv * 4) = ls;
#pragma unroll
    for (int j = 0; j < 4; ++j) pa[j] = cvtpk(p[2 * j], p[2 * j + 1]);
  }
  STAMP(7);
  SB();
  const rsrc_t rwo = mkrsrc(wo + (size_t)ri * 64 * DM + part * 128, 64 * DM * 2);
  const rsrc_t rwd = mkrsrc(wd + (size_t)(ffn ? fr : 0) * 32 * DM + part * 128, ffn ? 32 * DM * 2 : 0);
  v4u bw[3];
  bw[0] = __builtin_amdgcn_raw_buffer_load_b128(rwd, (4 * wv + g) * 1024 + c * 16, 0, AUX_STREAM);
  bw[1] = __builtin_amdgcn_raw_buffer_load_b128(rwo, (8 * wv + g) * 1024 + c * 16, 0, AUX_STREAM);
  bw[2] = __builtin_amdgcn_raw_buffer_load_b128(rwo, (8 * wv + g) * 1024 + c * 16, 4096, AUX_STREAM);
  SB();

  {
    float o[8];
#pragma unroll
    for (int dd = 0; dd < 8; ++dd) {
      v4u b;
#pragma unroll
      for (int jj = 0; jj < 4; ++jj) b[jj] = (dd & 1) ? phi(vv[2 * jj + 1][dd >> 1], vv[2 * jj][dd >> 1]) : plo(vv[2 * jj + 1][dd >> 1], vv[2 * jj][dd >> 1]);
      const f32x4 r = mfma(pa, b, f32x4{0.f, 0.f, 0.f, 0.f});
      o[dd] = r[0];
    }
    *(LAS f32x4*)(L + L_RED + wv * 2048 + g * 256 + c * 16) = f32x4{o[0], o[1], o[2], o[3]};
    *(LAS f32x4*)(L + L_RED + wv * 2048 + 1024 + g * 256 + c * 16) = f32x4{o[4], o[5], o[6], o[7]};
  }
  STAMP(8);
  BAR();
  {
    const int plane = lane >> 5, cc = (lane >> 1) & 15, lo = lane & 1;
    const int ra = wv < 4 ? L_RED + plane * 1024 + wv * 256 + cc * 16 + lo * 8 : L_M + (wv - 4) * 32;
    float a0, a1;
    if (wv < 4) {
      a0 = 0.f; a1 = 0.f;
#pragma unroll
      for (int k = 0; k < 8; ++k) { const f32x2 v = *(LAS f32x2*)(L + ra + k * 2048); a0 += v.x; a1 += v.y; }
    } else {
      const f32x4 m0 = *(LAS f32x4*)(L + ra), m1 = *(LAS f32x4*)(L + ra + 16);
      const f32x4 l0 = *(LAS f32x4*)(L + ra + 128), l1 = *(LAS f32x4*)(L + ra + 144);
      a0 = fmaxf(fmaxf(fmaxf(m0[0], m0[1]), fmaxf(m0[2], m0[3])), fmaxf(fmaxf(m1[0], m1[1]), fmaxf(m1[2], m1[3]))) * C2;
      a1 = ((l0[0] + l0[1]) + (l0[2] + l0[3])) + ((l1[0] + l1[1]) + (l1[2] + l1[3]));
    }
    const int off = wv < 4 ? WS_OP + ((((xcd * 4 + wv) * 2 + (cc >> 3)) * 8 + grp) * 4 + part) * 512 + ((cc & 7) * 8 + plane * 4 + lo * 2) * 8
                           : (lane == 0 ? WS_ML + (((xcd * 4 + wv - 4) * 8 + grp) * 4 + part) * 16 : WS_END);
    __builtin_amdgcn_raw_buffer_store_b128(unit(a0, a1, tag), rws, off, 0, AUX_SC1);
  }
  STAMP(9);
  SB();

  const int obase = WS_OP + ((ri * 8 + wv) * 4 + g) * 512 + c * 16;
  const int mlbase = WS_ML + (((ri >> 1) * 8 + wv) * 4 + g) * 16;
  v4u ou0 = __builtin_amdgcn_raw_buffer_load_b128(rws, obase, 0, AUX_SC1);
  v4u ou1 = __builtin_amdgcn_raw_buffer_load_b128(rws, obase + 256, 0, AUX_SC1);
  v4u mlu = __builtin_amdgcn_raw_buffer_load_b128(rws, mlbase, 0, AUX_SC1);
  SB();
  *(LAS v4u*)(L + L_K + offb(64 + 4 * wv + g, c)) = bw[0];
  *(LAS v4u*)(L + L_K + offb(8 * wv + g, c)) = bw[1];
  *(LAS v4u*)(L + L_K + offb(8 * wv + 4 + g, c)) = bw[2];
  STAMP(10);
  BAR();
  v4u af[3];
#pragma unroll
  for (int ks = 0; ks < 3; ++ks) {
    const v2u lo = trread(L + L_K + offb(32 * ks + 8 * g + (c >> 2), 2 * wv + ((c & 3) >> 1)) + 8 * (c & 1));
    const v2u hi = trread(L + L_K + offb(32 * ks + 8 * g + 4 + (c >> 2), 2 * wv + ((c & 3) >> 1)) + 8 * (c & 1));
    af[ks] = v4u{lo.x, lo.y, hi.x, hi.y};
  }
  const int sfoff = WS_SF + ((c & 7) * 32 + part * 8 + wv) * 15360 + (2 * g + (up ? 1 : 0)) * 16;
  {
    const v4u bh = *(LAS v4u*)(L + L_H + (c & 7) * 96 + g * 16);
    const f32x4 d = mfma(af[2], bh, f32x4{0.f, 0.f, 0.f, 0.f});
    __builtin_amdgcn_raw_buffer_store_b128(unit(up ? d[2] : d[0], up ? d[3] : d[1], tag), rws, ffn ? sfoff + (64 + fr) * 128 : WS_END, 0, AUX_SC1);
  }
  SB();
  {
    bool bad = !(uok(ou0, tag) & uok(ou1, tag) & uok(mlu, tag));
    if (__builtin_expect(__any(bad), 0)) {
      unsigned spin = 0;
      do {
        __builtin_amdgcn_s_sleep(POLL_SLEEP);
        if (bad) repoll3(ou0, ou1, mlu, rws, obase, mlbase);
        bad = !(uok(ou0, tag) & uok(ou1, tag) & uok(mlu, tag));
      } while (__any(bad) && ++spin < SPIN_CAP);
      nsp1 = spin + 1;
    }
  }
  STAMP(11);
  {
    const float ms = __uint_as_float(mlu.x), lsp = __uint_as_float(mlu.z);
    const float M = xrow_max(ms);
    const float e = __builtin_amdgcn_exp2f(ms - M);
    const float Lt = xrow_sum(e * lsp);
    const float wgt = e * __builtin_amdgcn_rcpf(Lt);
    const float o0 = xrow_sum(wgt * __uint_as_float(ou0.x)), o1 = xrow_sum(wgt * __uint_as_float(ou0.z));
    const float o2 = xrow_sum(wgt * __uint_as_float(ou1.x)), o3 = xrow_sum(wgt * __uint_as_float(ou1.z));
    const unsigned pk = cvtpk(g == 0 ? o0 : o2, g == 0 ? o1 : o3);
    if (g < 2) *(LAS unsigned*)(L + L_O16 + wv * 160 + g * 64 + c * 4) = pk;
  }
  BAR();
  {
    const v4u bo0 = *(LAS v4u*)(L + L_O16 + (c & 7) * 160 + g * 16), bo1 = *(LAS v4u*)(L + L_O16 + (c & 7) * 160 + 64 + g * 16);
    f32x4 d = mfma(af[0], bo0, f32x4{0.f, 0.f, 0.f, 0.f});
    d = mfma(af[1], bo1, d);
    __builtin_amdgcn_raw_buffer_store_b128(unit(up ? d[2] : d[0], up ? d[3] : d[1], tag), rws, sfoff + ri * 128, 0, AUX_SC1);
  }
  STAMP(12);
  SB();

  {
    const int line0 = wv < 4 ? 16 * wv : 64 + 14 * (wv - 4);
    const int fa = WS_SF + w * 15360 + line0 * 128 + lane * 16;
    const bool two = (wv < 4) | (lane < 48);
    const int fb = two ? fa + 1024 : fa;
    v4u f0 = __builtin_amdgcn_raw_buffer_load_b128(rws, fa, 0, AUX_SC1);
    v4u f1 = __builtin_amdgcn_raw_buffer_load_b128(rws, fb, 0, AUX_SC1);
    bool bad = !(uok(f0, tag) & uok(f1, tag));
    if (__any(bad)) {
      unsigned spin = 0;
      do {
        __builtin_amdgcn_s_sleep(POLL_SLEEP);
        if (bad) repoll2(f0, f1, rws, fa, fb);
        bad = !(uok(f0, tag) & uok(f1, tag));
      } while (__any(bad) && ++spin < SPIN_CAP);
      nsp2 = spin + 1;
    }
    STAMP(13);
    float a0 = __uint_as_float(f0.x) + (two ? __uint_as_float(f1.x) : 0.f);
    float a1 = __uint_as_float(f0.z) + (two ? __uint_as_float(f1.z) : 0.f);
    a0 += dpp<XOR8>(a0); a1 += dpp<XOR8>(a1);
    a0 = xrow_sum(a0); a1 = xrow_sum(a1);
    if (lane < 8) *(LAS f32x2*)(L + L_FIN + wv * 64 + lane * 8) = f32x2{a0, a1};
  }
  BAR();
  if (wv == 0 && lane < 16) {
    float s = 0.f, f = 0.f;
#pragma unroll
    for (int k = 0; k < 4; ++k) { s += *(LAS float*)(L + L_FIN + k * 64 + lane * 4); f += *(LAS float*)(L + L_FIN + (4 + k) * 64 + lane * 4); }
    const float gt = rbf(silu(rbf(f)));
    const float y = bf_lo(xfin) + rbf(rbf(s) * gt);
    out[w * 16 + lane] = (u16)cvtpk(y, y);
    if (w == 0 && lane == 0) __builtin_amdgcn_raw_buffer_store_b32(tag, rws, 0, WS_EPOCH, AUX_SC1);
  }
  STAMP(14);
  if (wv == 0) { const u64 d = st[LEAK] - st[0]; while (__builtin_amdgcn_s_memrealtime() - st[14] < d) __builtin_amdgcn_s_sleep(1); }
  if (tid == 12345) {
    u64* sp = (u64*)(ws + WS_STAMP) + w * 16;
#pragma unroll
    for (int i = 0; i < 15; ++i) sp[i] = st[i];
    sp[15] = (u64)nsp0 | ((u64)nsp1 << 20) | ((u64)nsp2 << 40);
  }
}

extern "C" void kernel_launch(void* const* d_in, const int* in_sizes, int n_in, void* d_out, int out_size, void* d_ws, size_t ws_size,
                              hipStream_t stream) {
  fused_block<<<256, 512, 0, stream>>>((const u16*)d_in[0], (const u16*)d_in[1], (const u16*)d_in[2], (const u16*)d_in[3], (const u16*)d_in[4],
                                       (const u16*)d_in[6], (const u16*)d_in[7], (const u16*)d_in[8], (u16*)d_out, (unsigned char*)d_ws);
}
```

```cpp
#define SH 40
#define STAMPS 1
#define LEAK 0
#include <hip/hip_runtime.h>
#include <stdint.h>

#define AUX_STREAM 2

typedef unsigned short u16;
typedef unsigned long long u64;
typedef unsigned v4u __attribute__((ext_vector_type(4)));
typedef unsigned v2u __attribute__((ext_vector_type(2)));
typedef float f32x4 __attribute__((ext_vector_type(4)));
typedef float f32x2 __attribute__((ext_vector_type(2)));
typedef short bf16x8 __attribute__((ext_vector_type(8)));
typedef __amdgpu_buffer_rsrc_t rsrc_t;
#define LAS __attribute__((address_space(3)))
#define GAS __attribute__((address_space(1)))

constexpr int NB = 8, DM = 512, HQ = 32, HKV = 8, SK = 4096, DH = 128, WIN = 1024, FF = 1792;
constexpr int AUX_SC1 = 16;
constexpr int RSRC_W3 = 0x00020000;
constexpr unsigned SPIN_CAP = 1u << 18;

constexpr int WS_EPOCH = 0;
constexpr int WS_ML = 4096;
constexpr int WS_QP = 32768;
constexpr int WS_GU = WS_QP + (1 << 20);
constexpr int WS_OP = WS_GU + 56 * 16384;
constexpr int WS_SF = WS_OP + (1 << 20);
constexpr int WS_END = WS_SF + 256 * 15360;
constexpr int WS_STAMP = 8 << 20;

constexpr int L_K = 0;
constexpr int L_RED = 65536;
constexpr int L_XN = L_RED + 8 * 8 * 272;
constexpr int L_Q = L_XN + 8 * 288;
constexpr int L_H = L_Q + 4 * 288;
constexpr int L_M = L_H + 8 * 96;
constexpr int L_L = L_M + 128;
constexpr int L_O16 = L_L + 128;
constexpr int L_FIN = L_O16 + 8 * 160;
constexpr int L_END = L_FIN + 512;

__device__ __forceinline__ float bf_lo(unsigned u) { return __uint_as_float(u << 16); }
__device__ __forceinline__ float bf_hi(unsigned u) { return __uint_as_float(u & 0xffff0000u); }
__device__ __forceinline__ unsigned cvtpk(float lo, float hi) {
  typedef __bf16 bf16x2 __attribute__((ext_vector_type(2)));
  return __builtin_bit_cast(unsigned, __builtin_convertvector(f32x2{lo, hi}, bf16x2));
}
__device__ __forceinline__ float rbf(float f) { return bf_lo(cvtpk(f, f)); }
template <int CTRL> __device__ __forceinline__ float dpp(float x) {
  return __builtin_bit_cast(float, __builtin_amdgcn_mov_dpp(__builtin_bit_cast(int, x), CTRL, 0xf, 0xf, true));
}
constexpr int XOR1 = 0xB1, XOR2 = 0x4E, XOR7 = 0x141, XOR8 = 0x128;
__device__ __forceinline__ float xrow_sum(float x) {
  auto s = __builtin_amdgcn_permlane16_swap(__float_as_uint(x), __float_as_uint(x), false, false);
  x = __uint_as_float(s[0]) + __uint_as_float(s[1]);
  auto t = __builtin_amdgcn_permlane32_swap(__float_as_uint(x), __float_as_uint(x), false, false);
  return __uint_as_float(t[0]) + __uint_as_float(t[1]);
}
__device__ __forceinline__ float xrow_max(float x) {
  auto s = __builtin_amdgcn_permlane16_swap(__float_as_uint(x), __float_as_uint(x), false, false);
  x = fmaxf(__uint_as_float(s[0]), __uint_as_float(s[1]));
  auto t = __builtin_amdgcn_permlane32_swap(__float_as_uint(x), __float_as_uint(x), false, false);
  return fmaxf(__uint_as_float(t[0]), __uint_as_float(t[1]));
}
__device__ __forceinline__ float wave_sum(float x) {
  x += dpp<XOR1>(x); x += dpp<XOR2>(x); x += dpp<XOR7>(x); x += dpp<XOR8>(x);
  return xrow_sum(x);
}
__device__ __forceinline__ f32x4 mfma(v4u a, v4u b, f32x4 c) {
  return __builtin_amdgcn_mfma_f32_16x16x32_bf16(__builtin_bit_cast(bf16x8, a), __builtin_bit_cast(bf16x8, b), c, 0, 0, 0);
}
__device__ __forceinline__ unsigned plo(unsigned r1, unsigned r0) { return __builtin_amdgcn_perm(r1, r0, 0x05040100u); }
__device__ __forceinline__ unsigned phi(unsigned r1, unsigned r0) { return __builtin_amdgcn_perm(r1, r0, 0x07060302u); }
__device__ __forceinline__ rsrc_t mkrsrc(const void* p, unsigned bytes) { return __builtin_amdgcn_make_buffer_rsrc((void*)p, 0, bytes, RSRC_W3); }
__device__ __forceinline__ v4u unit(float a, float b, unsigned tag) { return v4u{__float_as_uint(a), tag, __float_as_uint(b), tag}; }
__device__ __forceinline__ bool uok(v4u u, unsigned tag) { return (u.y == tag) & (u.w == tag); }
__device__ __forceinline__ float silu(float x) { return x * __builtin_amdgcn_rcpf(1.f + __builtin_amdgcn_exp2f(-1.4426950408889634f * x)); }
#define SB() __builtin_amdgcn_sched_barrier(0)
#define POLL_SLEEP 1
__device__ __forceinline__ void repoll2(v4u& a, v4u& b, rsrc_t rs, int oa, int ob) {
  asm volatile("buffer_load_dwordx4 %0, %2, %4, 0 offen sc1\n\tbuffer_load_dwordx4 %1, %3, %4, 0 offen sc1\n\ts_waitcnt vmcnt(0)"
               : "+v"(a), "+v"(b) : "v"(oa), "v"(ob), "s"(rs) : "memory");
}
__device__ __forceinline__ void repoll3(v4u& a, v4u& b, v4u& c, rsrc_t rs, int oa, int oc) {
  asm volatile("buffer_load_dwordx4 %0, %3, %5, 0 offen sc1\n\tbuffer_load_dwordx4 %1, %3, %5, 0 offen offset:256 sc1\n\t"
               "buffer_load_dwordx4 %2, %4, %5, 0 offen sc1\n\ts_waitcnt vmcnt(0)"
               : "+v"(a), "+v"(b), "+v"(c) : "v"(oa), "v"(oc), "s"(rs) : "memory");
}
__device__ __forceinline__ void repoll4(v4u& a, v4u& b, v4u& c, v4u& d, rsrc_t rs, int oa, int ob, int oc, int od) {
  asm volatile("buffer_load_dwordx4 %0, %4, %8, 0 offen sc1\n\tbuffer_load_dwordx4 %1, %5, %8, 0 offen sc1\n\t"
               "buffer_load_dwordx4 %2, %6, %8, 0 offen sc1\n\tbuffer_load_dwordx4 %3, %7, %8, 0 offen sc1\n\ts_waitcnt vmcnt(0)"
               : "+v"(a), "+v"(b), "+v"(c), "+v"(d) : "v"(oa), "v"(ob), "v"(oc), "v"(od), "s"(rs) : "memory");
}
typedef short s16x4 __attribute__((ext_vector_type(4)));
__device__ __forceinline__ v2u trread(LAS unsigned char* p) { return __builtin_bit_cast(v2u, __builtin_amdgcn_ds_read_tr16_b64_v4i16((LAS s16x4*)p)); }
__device__ __forceinline__ int offb(int row, int ch) { return 256 * row + 16 * (ch ^ (((row & 3) << 2) | ((row >> 2) & 3))); }
#define BAR() do { asm volatile("s_waitcnt lgkmcnt(0)" ::: "memory"); __builtin_amdgcn_s_barrier(); asm volatile("" ::: "memory"); } while (0)
template <typename T> __device__ __forceinline__ T ldnt(const T* p) { return AUX_STREAM == 2 ? __builtin_nontemporal_load(p) : *p; }
#define STAMP(i) do { SB(); st[i] = __builtin_amdgcn_s_memrealtime(); SB(); } while (0)

__global__ void __launch_bounds__(512, 2)
fused_block(const u16* __restrict__ x, const u16* __restrict__ kc, const u16* __restrict__ vc, const u16* __restrict__ wq,
            const u16* __restrict__ wo, const u16* __restrict__ wg, const u16* __restrict__ wu, const u16* __restrict__ wd,
            u16* __restrict__ out, unsigned char* ws) {
  __shared__ __attribute__((aligned(16))) unsigned char lds_raw[L_END];
  LAS unsigned char* L = (LAS unsigned char*)lds_raw;
  u64 st[16];
#pragma unroll
  for (int i = 0; i < 16; ++i) st[i] = 0;
  unsigned nsp0 = 0, nsp1 = 0, nsp2 = 0;
  STAMP(0);
  const int tid = threadIdx.x, lane = tid & 63, wv = __builtin_amdgcn_readfirstlane(tid >> 6);
  const int g = lane >> 4, c = lane & 15;
  const int w = blockIdx.x, xcd = w & 7, idx = w >> 3, part = idx & 3, grp = idx >> 2;
  const bool ffn = idx < 28;
  const int fr = xcd * 7 + grp;
  const int ri = xcd * 8 + grp;
  const rsrc_t rws = mkrsrc(ws, WS_END);
  const rsrc_t rx = mkrsrc(x, NB * DM * 2);
  const size_t kvoff = ((size_t)(grp * HKV + xcd) * SK + (SK - WIN) + part * 256) * DH;
  const rsrc_t rk = mkrsrc(kc + kvoff, 65536);
  const rsrc_t rv = mkrsrc(vc + kvoff, 65536);
  const bool front = (wv < 4) | ffn;

  const v4u xr = __builtin_amdgcn_raw_buffer_load_b128(rx, lane * 16, wv * 1024, 0);
  const unsigned ep = __builtin_amdgcn_raw_buffer_load_b32(rws, 0, WS_EPOCH, AUX_SC1);
  const unsigned xfin = __builtin_amdgcn_raw_buffer_load_b16(rx, (w * 16 + c) * 2, 0, 0);
  v2u fw[8];
  {
    const int krow = part * 128 + (wv & 3) * 32 + 8 * g;
    const u16* p = wv < 4 ? wq + (size_t)krow * (HQ * DH) + xcd * 512 + grp * 64 + 4 * c
                          : (ffn ? (c < 8 ? wg : wu) + (size_t)krow * FF + fr * 32 + (c & 7) * 4 : x + 4 * c);
    const int stride = wv < 4 ? HQ * DH : (ffn ? FF : 0);
#pragma unroll
    for (int j = 0; j < 8; ++j) fw[j] = ldnt((const v2u*)(p + j * stride));
  }
  SB();
  v4u kv[8];
#pragma unroll
  for (int t = 0; t < 8; ++t) kv[t] = __builtin_amdgcn_raw_buffer_load_b128(rk, wv * 8192 + lane * 16, t * 1024, AUX_STREAM);
  SB();

  {
    float v[8];
#pragma unroll
    for (int j = 0; j < 4; ++j) { v[2 * j] = bf_lo(xr[j]); v[2 * j + 1] = bf_hi(xr[j]); }
    float s = ((v[0] + v[1]) + (v[2] + v[3])) + ((v[4] + v[5]) + (v[6] + v[7]));
    const float mean = wave_sum(s) * (1.f / DM);
    float q = 0.f;
#pragma unroll
    for (int j = 0; j < 8; ++j) { const float d = v[j] - mean; q = fmaf(d, d, q); }
    const float var = rbf(wave_sum(q) * (1.f / DM));
    const float mb = rbf(mean);
    const float rden = __builtin_amdgcn_rcpf(rbf(__builtin_amdgcn_sqrtf(rbf(var + 1e-5f))));
    v4u o;
#pragma unroll
    for (int j = 0; j < 4; ++j) {
      const unsigned dpk = cvtpk(v[2 * j] - mb, v[2 * j + 1] - mb);
      o[j] = cvtpk(bf_lo(dpk) * rden, bf_hi(dpk) * rden);
    }
    if (g == part) *(LAS v4u*)(L + L_XN + wv * 288 + c * 16) = o;
  }
  STAMP(1);
  BAR();
  const unsigned tag = __builtin_amdgcn_readfirstlane(ep) + 1u;
  const bool up = c >= 8;

  {
    const v4u bx = *(LAS v4u*)(L + L_XN + (c & 7) * 288 + (wv & 3) * 64 + g * 16);
    f32x4 d[4];
#pragma unroll
    for (int dd = 0; dd < 4; ++dd) {
      v4u a;
#pragma unroll
      for (int jj = 0; jj < 4; ++jj) a[jj] = (dd & 1) ? phi(fw[2 * jj + 1][dd >> 1], fw[2 * jj][dd >> 1]) : plo(fw[2 * jj + 1][dd >> 1], fw[2 * jj][dd >> 1]);
      d[dd] = mfma(a, bx, f32x4{0.f, 0.f, 0.f, 0.f});
    }
#pragma unroll
    for (int rr = 0; rr < 2; ++rr) {
      f32x4 o;
#pragma unroll
      for (int dd = 0; dd < 4; ++dd) o[dd] = up ? d[dd][rr + 2] : d[dd][rr];
      *(LAS f32x4*)(L + L_RED + (wv * 8 + (c & 7)) * 272 + g * 64 + (rr + (up ? 2 : 0)) * 16) = o;
    }
  }
  STAMP(2);
  BAR();
  const rsrc_t rfr = mkrsrc(ws, front ? WS_END : 0);
  {
    const int t = tid & 255, b = t >> 5, pr = t & 31;
    const int ra = L_RED + ((wv & 4) * 8 + b) * 272 + (wv < 4 ? pr * 8 : pr * 4), rb = ra + (wv < 4 ? 4 : 128);
    float a0 = 0.f, a1 = 0.f;
#pragma unroll
    for (int k = 0; k < 4; ++k) { a0 += *(LAS float*)(L + ra + k * 8 * 272); a1 += *(LAS float*)(L + rb + k * 8 * 272); }
    const int off = wv < 4 ? WS_QP + ((xcd * 8 + b) * 4 + part) * 4096 + grp * 512 + pr * 16 : WS_GU + (fr * 4 + part) * 4096 + t * 16;
    __builtin_amdgcn_raw_buffer_store_b128(unit(a0, a1, tag), rfr, off, 0, AUX_SC1);
  }
  STAMP(3);
  SB();

  LAS unsigned char* Kw = L + L_K + wv * 8192;
#pragma unroll
  for (int t = 0; t < 4; ++t) *(LAS v4u*)(Kw + (4 * t + g) * 256 + ((c ^ (4 * t + g)) * 16)) = kv[t];
  SB();
  STAMP(4);
  const int goff = (wv < 4 ? WS_QP + (xcd * 8 + grp) * 16384 : WS_GU + fr * 16384) + (tid & 255) * 16;
  v4u pu[4];
#pragma unroll
  for (int k = 0; k < 4; ++k) pu[k] = __builtin_amdgcn_raw_buffer_load_b128(rfr, goff, k * 4096, AUX_SC1);
  SB();
  v4u vv[8];
#pragma unroll
  for (int t = 0; t < 4; ++t) vv[t] = __builtin_amdgcn_raw_buffer_load_b128(rv, wv * 8192 + g * 1024 + c * 16 + t * 256, 0, AUX_STREAM);
  SB();
#pragma unroll
  for (int t = 4; t < 8; ++t) *(LAS v4u*)(Kw + (4 * t + g) * 256 + ((c ^ ((4 * t + g) & 15)) * 16)) = kv[t];
  SB();
#pragma unroll
  for (int t = 4; t < 8; ++t) vv[t] = __builtin_amdgcn_raw_buffer_load_b128(rv, wv * 8192 + g * 1024 + c * 16 + (t - 4) * 256, 4096, AUX_STREAM);
  SB();
  STAMP(5);

  {
    bool bad = front & !(uok(pu[0], tag) & uok(pu[1], tag) & uok(pu[2], tag) & uok(pu[3], tag));
    if (__builtin_expect(__any(bad), 0)) {
      unsigned spin = 0;
      do {
        __builtin_amdgcn_s_sleep(POLL_SLEEP);
        if (bad) repoll4(pu[0], pu[1], pu[2], pu[3], rws, goff, goff + 4096, goff + 8192, goff + 12288);
        bad = front & !(uok(pu[0], tag) & uok(pu[1], tag) & uok(pu[2], tag) & uok(pu[3], tag));
      } while (__any(bad) && ++spin < SPIN_CAP);
      nsp0 = spin + 1;
    }
    const float a0 = (__uint_as_float(pu[0].x) + __uint_as_float(pu[1].x)) + (__uint_as_float(pu[2].x) + __uint_as_float(pu[3].x));
    const float a1 = (__uint_as_float(pu[0].z) + __uint_as_float(pu[1].z)) + (__uint_as_float(pu[2].z) + __uint_as_float(pu[3].z));
    const unsigned pk = cvtpk(a0, a1);
    if (wv < 4) {
      *(LAS unsigned*)(L + L_Q + wv * 288 + lane * 4) = pk;
    } else {
      const float sg = rbf(silu(bf_lo(pk)));
      const unsigned hb = cvtpk(sg * bf_hi(pk), 0.f);
      const int t = tid & 255;
      *(LAS u16*)(L + L_H + (t >> 5) * 96 + (t & 31) * 2) = (u16)hb;
    }
  }
  STAMP(6);
  BAR();

  constexpr float C2 = 0.08838834764831845f * 1.4426950408889634f;
  f32x4 s0 = {0.f, 0.f, 0.f, 0.f}, s1 = {0.f, 0.f, 0.f, 0.f};
#pragma unroll
  for (int i = 0; i < 4; ++i) {
    const v4u bq = *(LAS v4u*)(L + L_Q + (c >> 2) * 288 + i * 64 + g * 16);
    const v4u a0 = *(LAS v4u*)(Kw + c * 256 + (((4 * i + g) ^ c) * 16));
    const v4u a1 = *(LAS v4u*)(Kw + 4096 + c * 256 + (((4 * i + g) ^ c) * 16));
    s0 = mfma(a0, bq, s0);
    s1 = mfma(a1, bq, s1);
  }
  {
    float mx = fmaxf(fmaxf(fmaxf(s0[0], s0[1]), fmaxf(s0[2], s0[3])), fmaxf(fmaxf(s1[0], s1[1]), fmaxf(s1[2], s1[3])));
    mx = xrow_max(mx);
    if ((lane & 0x33) == 0) *(LAS float*)(L + L_M + (c >> 2) * 32 + wv * 4) = mx;
  }
  BAR();
  v4u pa;
  {
    const f32x4 m0 = *(LAS f32x4*)(L + L_M + (c >> 2) * 32), m1 = *(LAS f32x4*)(L + L_M + (c >> 2) * 32 + 16);
    const float M = fmaxf(fmaxf(fmaxf(m0[0], m0[1]), fmaxf(m0[2], m0[3])), fmaxf(fmaxf(m1[0], m1[1]), fmaxf(m1[2], m1[3])));
    const float mc = M * C2;
    float p[8];
#pragma unroll
    for (int r = 0; r < 4; ++r) { p[r] = __builtin_amdgcn_exp2f(fmaf(s0[r], C2, -mc)); p[4 + r] = __builtin_amdgcn_exp2f(fmaf(s1[r], C2, -mc)); }
    float ls = ((p[0] + p[1]) + (p[2] + p[3])) + ((p[4] + p[5]) + (p[6] + p[7]));
    ls = xrow_sum(ls);
    if ((lane & 0x33) == 0) *(LAS float*)(L + L_L + (c >> 2) * 32 + wv * 4) = ls;
#pragma unroll
    for (int j = 0; j < 4; ++j) pa[j] = cvtpk(p[2 * j], p[2 * j + 1]);
  }
  STAMP(7);
  SB();
  const rsrc_t rwo = mkrsrc(wo + (size_t)ri * 64 * DM + part * 128, 64 * DM * 2);
  const rsrc_t rwd = mkrsrc(wd + (size_t)(ffn ? fr : 0) * 32 * DM + part * 128, ffn ? 32 * DM * 2 : 0);
  v4u bw[3];
  bw[0] = __builtin_amdgcn_raw_buffer_load_b128(rwd, (4 * wv + g) * 1024 + c * 16, 0, AUX_STREAM);
  bw[1] = __builtin_amdgcn_raw_buffer_load_b128(rwo, (8 * wv + g) * 1024 + c * 16, 0, AUX_STREAM);
  bw[2] = __builtin_amdgcn_raw_buffer_load_b128(rwo, (8 * wv + g) * 1024 + c * 16, 4096, AUX_STREAM);
  SB();

  {
    float o[8];
#pragma unroll
    for (int dd = 0; dd < 8; ++dd) {
      v4u b;
#pragma unroll
      for (int jj = 0; jj < 4; ++jj) b[jj] = (dd & 1) ? phi(vv[2 * jj + 1][dd >> 1], vv[2 * jj][dd >> 1]) : plo(vv[2 * jj + 1][dd >> 1], vv[2 * jj][dd >> 1]);
      const f32x4 r = mfma(pa, b, f32x4{0.f, 0.f, 0.f, 0.f});
      o[dd] = r[0];
    }
    *(LAS f32x4*)(L + L_RED + wv * 2048 + g * 256 + c * 16) = f32x4{o[0], o[1], o[2], o[3]};
    *(LAS f32x4*)(L + L_RED + wv * 2048 + 1024 + g * 256 + c * 16) = f32x4{o[4], o[5], o[6], o[7]};
  }
  STAMP(8);
  BAR();
  {
    const int plane = lane >> 5, cc = (lane >> 1) & 15, lo = lane & 1;
    const int ra = wv < 4 ? L_RED + plane * 1024 + wv * 256 + cc * 16 + lo * 8 : L_M + (wv - 4) * 32;
    float a0, a1;
    if (wv < 4) {
      a0 = 0.f; a1 = 0.f;
#pragma unroll
      for (int k = 0; k < 8; ++k) { const f32x2 v = *(LAS f32x2*)(L + ra + k * 2048); a0 += v.x; a1 += v.y; }
    } else {
      const f32x4 m0 = *(LAS f32x4*)(L + ra), m1 = *(LAS f32x4*)(L + ra + 16);
      const f32x4 l0 = *(LAS f32x4*)(L + ra + 128), l1 = *(LAS f32x4*)(L + ra + 144);
      a0 = fmaxf(fmaxf(fmaxf(m0[0], m0[1]), fmaxf(m0[2], m0[3])), fmaxf(fmaxf(m1[0], m1[1]), fmaxf(m1[2], m1[3]))) * C2;
      a1 = ((l0[0] + l0[1]) + (l0[2] + l0[3])) + ((l1[0] + l1[1]) + (l1[2] + l1[3]));
    }
    const int off = wv < 4 ? WS_OP + ((((xcd * 4 + wv) * 2 + (cc >> 3)) * 8 + grp) * 4 + part) * 512 + ((cc & 7) * 8 + plane * 4 + lo * 2) * 8
                           : (lane == 0 ? WS_ML + (((xcd * 4 + wv - 4) * 8 + grp) * 4 + part) * 16 : WS_END);
    __builtin_amdgcn_raw_buffer_store_b128(unit(a0, a1, tag), rws, off, 0, AUX_SC1);
  }
  STAMP(9);
  SB();

  const int obase = WS_OP + ((ri * 8 + wv) * 4 + g) * 512 + c * 16;
  const int mlbase = WS_ML + (((ri >> 1) * 8 + wv) * 4 + g) * 16;
  v4u ou0 = __builtin_amdgcn_raw_buffer_load_b128(rws, obase, 0, AUX_SC1);
  v4u ou1 = __builtin_amdgcn_raw_buffer_load_b128(rws, obase + 256, 0, AUX_SC1);
  v4u mlu = __builtin_amdgcn_raw_buffer_load_b128(rws, mlbase, 0, AUX_SC1);
  SB();
  *(LAS v4u*)(L + L_K + offb(64 + 4 * wv + g, c)) = bw[0];
  *(LAS v4u*)(L + L_K + offb(8 * wv + g, c)) = bw[1];
  *(LAS v4u*)(L + L_K + offb(8 * wv + 4 + g, c)) = bw[2];
  STAMP(10);
  BAR();
  v4u af[3];
#pragma unroll
  for (int ks = 0; ks < 3; ++ks) {
    const v2u lo = trread(L + L_K + offb(32 * ks + 8 * g + (c >> 2), 2 * wv + ((c & 3) >> 1)) + 8 * (c & 1));
    const v2u hi = trread(L + L_K + offb(32 * ks + 8 * g + 4 + (c >> 2), 2 * wv + ((c & 3) >> 1)) + 8 * (c & 1));
    af[ks] = v4u{lo.x, lo.y, hi.x, hi.y};
  }
  const int sfoff = WS_SF + ((c & 7) * 32 + part * 8 + wv) * 15360 + (2 * g + (up ? 1 : 0)) * 16;
  {
    const v4u bh = *(LAS v4u*)(L + L_H + (c & 7) * 96 + g * 16);
    const f32x4 d = mfma(af[2], bh, f32x4{0.f, 0.f, 0.f, 0.f});
    __builtin_amdgcn_raw_buffer_store_b128(unit(up ? d[2] : d[0], up ? d[3] : d[1], tag), rws, ffn ? sfoff + (64 + fr) * 128 : WS_END, 0, AUX_SC1);
  }
  SB();
  {
    bool bad = !(uok(ou0, tag) & uok(ou1, tag) & uok(mlu, tag));
    if (__builtin_expect(__any(bad), 0)) {
      unsigned spin = 0;
      do {
        __builtin_amdgcn_s_sleep(POLL_SLEEP);
        if (bad) repoll3(ou0, ou1, mlu, rws, obase, mlbase);
        bad = !(uok(ou0, tag) & uok(ou1, tag) & uok(mlu, tag));
      } while (__any(bad) && ++spin < SPIN_CAP);
      nsp1 = spin + 1;
    }
  }
  STAMP(11);
  {
    const float ms = __uint_as_float(mlu.x), lsp = __uint_as_float(mlu.z);
    const float M = xrow_max(ms);
    const float e = __builtin_amdgcn_exp2f(ms - M);
    const float Lt = xrow_sum(e * lsp);
    const float wgt = e * __builtin_amdgcn_rcpf(Lt);
    const float o0 = xrow_sum(wgt * __uint_as_float(ou0.x)), o1 = xrow_sum(wgt * __uint_as_float(ou0.z));
    const float o2 = xrow_sum(wgt * __uint_as_float(ou1.x)), o3 = xrow_sum(wgt * __uint_as_float(ou1.z));
    const unsigned pk = cvtpk(g == 0 ? o0 : o2, g == 0 ? o1 : o3);
    if (g < 2) *(LAS unsigned*)(L + L_O16 + wv * 160 + g * 64 + c * 4) = pk;
  }
  BAR();
  {
    const v4u bo0 = *(LAS v4u*)(L + L_O16 + (c & 7) * 160 + g * 16), bo1 = *(LAS v4u*)(L + L_O16 + (c & 7) * 160 + 64 + g * 16);
    f32x4 d = mfma(af[0], bo0, f32x4{0.f, 0.f, 0.f, 0.f});
    d = mfma(af[1], bo1, d);
    __builtin_amdgcn_raw_buffer_store_b128(unit(up ? d[2] : d[0], up ? d[3] : d[1], tag), rws, sfoff + ri * 128, 0, AUX_SC1);
  }
  STAMP(12);
  SB();

  {
    const int line0 = wv < 4 ? 16 * wv : 64 + 14 * (wv - 4);
    const int fa = WS_SF + w * 15360 + line0 * 128 + lane * 16;
    const bool two = (wv < 4) | (lane < 48);
    const int fb = two ? fa + 1024 : fa;
    v4u f0 = __builtin_amdgcn_raw_buffer_load_b128(rws, fa, 0, AUX_SC1);
    v4u f1 = __builtin_amdgcn_raw_buffer_load_b128(rws, fb, 0, AUX_SC1);
    bool bad = !(uok(f0, tag) & uok(f1, tag));
    if (__any(bad)) {
      unsigned spin = 0;
      do {
        __builtin_amdgcn_s_sleep(POLL_SLEEP);
        if (bad) repoll2(f0, f1, rws, fa, fb);
        bad = !(uok(f0, tag) & uok(f1, tag));
      } while (__any(bad) && ++spin < SPIN_CAP);
      nsp2 = spin + 1;
    }
    STAMP(13);
    float a0 = __uint_as_float(f0.x) + (two ? __uint_as_float(f1.x) : 0.f);
    float a1 = __uint_as_float(f0.z) + (two ? __uint_as_float(f1.z) : 0.f);
    a0 += dpp<XOR8>(a0); a1 += dpp<XOR8>(a1);
    a0 = xrow_sum(a0); a1 = xrow_sum(a1);
    if (lane < 8) *(LAS f32x2*)(L + L_FIN + wv * 64 + lane * 8) = f32x2{a0, a1};
  }
  BAR();
  if (wv == 0 && lane < 16) {
    float s = 0.f, f = 0.f;
#pragma unroll
    for (int k = 0; k < 4; ++k) { s += *(LAS float*)(L + L_FIN + k * 64 + lane * 4); f += *(LAS float*)(L + L_FIN + (4 + k) * 64 + lane * 4); }
    const float gt = rbf(silu(rbf(f)));
    const float y = bf_lo(xfin) + rbf(rbf(s) * gt);
    out[w * 16 + lane] = (u16)cvtpk(y, y);
    if (w == 0 && lane == 0) __builtin_amdgcn_raw_buffer_store_b32(tag, rws, 0, WS_EPOCH, AUX_SC1);
  }
  STAMP(14);
  if (wv == 0) { const u64 d = st[LEAK] - st[0]; while (__builtin_amdgcn_s_memrealtime() - st[14] < d) __builtin_amdgcn_s_sleep(1); }
  if (tid == 0 && w == 0) *(u64*)(ws + WS_STAMP) = st[14];
  if (tid == 12345) {
    u64* sp = (u64*)(ws + WS_STAMP) + w * 16;
#pragma unroll
    for (int i = 0; i < 15; ++i) sp[i] = st[i];
    sp[15] = (u64)nsp0 | ((u64)nsp1 << 20) | ((u64)nsp2 << 40);
  }
}

__global__ void post_kernel(unsigned char* ws) { u64 t = __builtin_amdgcn_s_memrealtime(); u64 d = t - *(volatile u64*)(ws + WS_STAMP); if (d > 5000) d = 5000; d = d >> SH; while (__builtin_amdgcn_s_memrealtime() - t < d) __builtin_amdgcn_s_sleep(1); }
extern "C" void kernel_launch(void* const* d_in, const int* in_sizes, int n_in, void* d_out, int out_size, void* d_ws, size_t ws_size,
                              hipStream_t stream) {
  fused_block<<<256, 512, 0, stream>>>((const u16*)d_in[0], (const u16*)d_in[1], (const u16*)d_in[2], (const u16*)d_in[3], (const u16*)d_in[4],
                                       (const u16*)d_in[6], (const u16*)d_in[7], (const u16*)d_in[8], (u16*)d_out, (unsigned char*)d_ws);
  post_kernel<<<1, 64, 0, stream>>>((unsigned char*)d_ws);
}
```

```cpp
#define STAMPS 1
#define LEAK 0
#include <hip/hip_runtime.h>
#include <stdint.h>

#define AUX_STREAM 2

typedef unsigned short u16;
typedef unsigned long long u64;
typedef unsigned v4u __attribute__((ext_vector_type(4)));
typedef unsigned v2u __attribute__((ext_vector_type(2)));
typedef float f32x4 __attribute__((ext_vector_type(4)));
typedef float f32x2 __attribute__((ext_vector_type(2)));
typedef short bf16x8 __attribute__((ext_vector_type(8)));
typedef __amdgpu_buffer_rsrc_t rsrc_t;
#define LAS __attribute__((address_space(3)))
#define GAS __attribute__((address_space(1)))

constexpr int NB = 8, DM = 512, HQ = 32, HKV = 8, SK = 4096, DH = 128, WIN = 1024, FF = 1792;
constexpr int AUX_SC1 = 16;
constexpr int RSRC_W3 = 0x00020000;
constexpr unsigned SPIN_CAP = 1u << 18;

constexpr int WS_EPOCH = 0;
constexpr int WS_ML = 4096;
constexpr int WS_QP = 32768;
constexpr int WS_GU = WS_QP + (1 << 20);
constexpr int WS_OP = WS_GU + 56 * 16384;
constexpr int WS_SF = WS_OP + (1 << 20);
constexpr int WS_END = WS_SF + 256 * 15360;
constexpr int WS_STAMP = 8 << 20;

constexpr int L_K = 0;
constexpr int L_RED = 65536;
constexpr int L_XN = L_RED + 8 * 8 * 272;
constexpr int L_Q = L_XN + 8 * 288;
constexpr int L_H = L_Q + 4 * 288;
constexpr int L_M = L_H + 8 * 96;
constexpr int L_L = L_M + 128;
constexpr int L_O16 = L_L + 128;
constexpr int L_FIN = L_O16 + 8 * 160;
constexpr int L_END = L_FIN + 512;

__device__ __forceinline__ float bf_lo(unsigned u) { return __uint_as_float(u << 16); }
__device__ __forceinline__ float bf_hi(unsigned u) { return __uint_as_float(u & 0xffff0000u); }
__device__ __forceinline__ unsigned cvtpk(float lo, float hi) {
  typedef __bf16 bf16x2 __attribute__((ext_vector_type(2)));
  return __builtin_bit_cast(unsigned, __builtin_convertvector(f32x2{lo, hi}, bf16x2));
}
__device__ __forceinline__ float rbf(float f) { return bf_lo(cvtpk(f, f)); }
template <int CTRL> __device__ __forceinline__ float dpp(float x) {
  return __builtin_bit_cast(float, __builtin_amdgcn_mov_dpp(__builtin_bit_cast(int, x), CTRL, 0xf, 0xf, true));
}
constexpr int XOR1 = 0xB1, XOR2 = 0x4E, XOR7 = 0x141, XOR8 = 0x128;
__device__ __forceinline__ float xrow_sum(float x) {
  auto s = __builtin_amdgcn_permlane16_swap(__float_as_uint(x), __float_as_uint(x), false, false);
  x = __uint_as_float(s[0]) + __uint_as_float(s[1]);
  auto t = __builtin_amdgcn_permlane32_swap(__float_as_uint(x), __float_as_uint(x), false, false);
  return __uint_as_float(t[0]) + __uint_as_float(t[1]);
}
__device__ __forceinline__ float xrow_max(float x) {
  auto s = __builtin_amdgcn_permlane16_swap(__float_as_uint(x), __float_as_uint(x), false, false);
  x = fmaxf(__uint_as_float(s[0]), __uint_as_float(s[1]));
  auto t = __builtin_amdgcn_permlane32_swap(__float_as_uint(x), __float_as_uint(x), false, false);
  return fmaxf(__uint_as_float(t[0]), __uint_as_float(t[1]));
}
__device__ __forceinline__ float wave_sum(float x) {
  x += dpp<XOR1>(x); x += dpp<XOR2>(x); x += dpp<XOR7>(x); x += dpp<XOR8>(x);
  return xrow_sum(x);
}
__device__ __forceinline__ f32x4 mfma(v4u a, v4u b, f32x4 c) {
  return __builtin_amdgcn_mfma_f32_16x16x32_bf16(__builtin_bit_cast(bf16x8, a), __builtin_bit_cast(bf16x8, b), c, 0, 0, 0);
}
__device__ __forceinline__ unsigned plo(unsigned r1, unsigned r0) { return __builtin_amdgcn_perm(r1, r0, 0x05040100u); }
__device__ __forceinline__ unsigned phi(unsigned r1, unsigned r0) { return __builtin_amdgcn_perm(r1, r0, 0x07060302u); }
__device__ __forceinline__ rsrc_t mkrsrc(const void* p, unsigned bytes) { return __builtin_amdgcn_make_buffer_rsrc((void*)p, 0, bytes, RSRC_W3); }
__device__ __forceinline__ v4u unit(float a, float b, unsigned tag) { return v4u{__float_as_uint(a), tag, __float_as_uint(b), tag}; }
__device__ __forceinline__ bool uok(v4u u, unsigned tag) { return (u.y == tag) & (u.w == tag); }
__device__ __forceinline__ float silu(float x) { return x * __builtin_amdgcn_rcpf(1.f + __builtin_amdgcn_exp2f(-1.4426950408889634f * x)); }
#define SB() __builtin_amdgcn_sched_barrier(0)
#define POLL_SLEEP 1
__device__ __forceinline__ void repoll2(v4u& a, v4u& b, rsrc_t rs, int oa, int ob) {
  asm volatile("buffer_load_dwordx4 %0, %2, %4, 0 offen sc1\n\tbuffer_load_dwordx4 %1, %3, %4, 0 offen sc1\n\ts_waitcnt vmcnt(0)"
               : "+v"(a), "+v"(b) : "v"(oa), "v"(ob), "s"(rs) : "memory");
}
__device__ __forceinline__ void repoll3(v4u& a, v4u& b, v4u& c, rsrc_t rs, int oa, int oc) {
  asm volatile("buffer_load_dwordx4 %0, %3, %5, 0 offen sc1\n\tbuffer_load_dwordx4 %1, %3, %5, 0 offen offset:256 sc1\n\t"
               "buffer_load_dwordx4 %2, %4, %5, 0 offen sc1\n\ts_waitcnt vmcnt(0)"
               : "+v"(a), "+v"(b), "+v"(c) : "v"(oa), "v"(oc), "s"(rs) : "memory");
}
__device__ __forceinline__ void repoll4(v4u& a, v4u& b, v4u& c, v4u& d, rsrc_t rs, int oa, int ob, int oc, int od) {
  asm volatile("buffer_load_dwordx4 %0, %4, %8, 0 offen sc1\n\tbuffer_load_dwordx4 %1, %5, %8, 0 offen sc1\n\t"
               "buffer_load_dwordx4 %2, %6, %8, 0 offen sc1\n\tbuffer_load_dwordx4 %3, %7, %8, 0 offen sc1\n\ts_waitcnt vmcnt(0)"
               : "+v"(a), "+v"(b), "+v"(c), "+v"(d) : "v"(oa), "v"(ob), "v"(oc), "v"(od), "s"(rs) : "memory");
}
typedef short s16x4 __attribute__((ext_vector_type(4)));
__device__ __forceinline__ v2u trread(LAS unsigned char* p) { return __builtin_bit_cast(v2u, __builtin_amdgcn_ds_read_tr16_b64_v4i16((LAS s16x4*)p)); }
__device__ __forceinline__ int offb(int row, int ch) { return 256 * row + 16 * (ch ^ (((row & 3) << 2) | ((row >> 2) & 3))); }
#define BAR() do { asm volatile("s_waitcnt lgkmcnt(0)" ::: "memory"); __builtin_amdgcn_s_barrier(); asm volatile("" ::: "memory"); } while (0)
template <typename T> __device__ __forceinline__ T ldnt(const T* p) { return AUX_STREAM == 2 ? __builtin_nontemporal_load(p) : *p; }
#define STAMP(i) do { SB(); st[i] = __builtin_amdgcn_s_memrealtime(); SB(); } while (0)

__global__ void __launch_bounds__(512, 2)
fused_block(const u16* __restrict__ x, const u16* __restrict__ kc, const u16* __restrict__ vc, const u16* __restrict__ wq,
            const u16* __restrict__ wo, const u16* __restrict__ wg, const u16* __restrict__ wu, const u16* __restrict__ wd,
            u16* __restrict__ out, unsigned char* ws) {
  __shared__ __attribute__((aligned(16))) unsigned char lds_raw[L_END];
  LAS unsigned char* L = (LAS unsigned char*)lds_raw;
  u64 st[16];
#pragma unroll
  for (int i = 0; i < 16; ++i) st[i] = 0;
  unsigned nsp0 = 0, nsp1 = 0, nsp2 = 0;
  STAMP(0);
  const int tid = threadIdx.x, lane = tid & 63, wv = __builtin_amdgcn_readfirstlane(tid >> 6);
  const int g = lane >> 4, c = lane & 15;
  const int w = blockIdx.x, xcd = w & 7, idx = w >> 3, part = idx & 3, grp = idx >> 2;
  const bool ffn = idx < 28;
  const int fr = xcd * 7 + grp;
  const int ri = xcd * 8 + grp;
  const rsrc_t rws = mkrsrc(ws, WS_END);
  const rsrc_t rx = mkrsrc(x, NB * DM * 2);
  const size_t kvoff = ((size_t)(grp * HKV + xcd) * SK + (SK - WIN) + part * 256) * DH;
  const rsrc_t rk = mkrsrc(kc + kvoff, 65536);
  const rsrc_t rv = mkrsrc(vc + kvoff, 65536);
  const bool front = (wv < 4) | ffn;

  const v4u xr = __builtin_amdgcn_raw_buffer_load_b128(rx, lane * 16, wv * 1024, 0);
  const unsigned ep = __builtin_amdgcn_raw_buffer_load_b32(rws, 0, WS_EPOCH, AUX_SC1);
  const unsigned xfin = __builtin_amdgcn_raw_buffer_load_b16(rx, (w * 16 + c) * 2, 0, 0);
  v2u fw[8];
  {
    const int krow = part * 128 + (wv & 3) * 32 + 8 * g;
    const u16* p = wv < 4 ? wq + (size_t)krow * (HQ * DH) + xcd * 512 + grp * 64 + 4 * c
                          : (ffn ? (c < 8 ? wg : wu) + (size_t)krow * FF + fr * 32 + (c & 7) * 4 : x + 4 * c);
    const int stride = wv < 4 ? HQ * DH : (ffn ? FF : 0);
#pragma unroll
    for (int j = 0; j < 8; ++j) fw[j] = ldnt((const v2u*)(p + j * stride));
  }
  SB();
  v4u kv[8];
#pragma unroll
  for (int t = 0; t < 8; ++t) kv[t] = __builtin_amdgcn_raw_buffer_load_b128(rk, wv * 8192 + lane * 16, t * 1024, AUX_STREAM);
  SB();

  {
    float v[8];
#pragma unroll
    for (int j = 0; j < 4; ++j) { v[2 * j] = bf_lo(xr[j]); v[2 * j + 1] = bf_hi(xr[j]); }
    float s = ((v[0] + v[1]) + (v[2] + v[3])) + ((v[4] + v[5]) + (v[6] + v[7]));
    const float mean = wave_sum(s) * (1.f / DM);
    float q = 0.f;
#pragma unroll
    for (int j = 0; j < 8; ++j) { const float d = v[j] - mean; q = fmaf(d, d, q); }
    const float var = rbf(wave_sum(q) * (1.f / DM));
    const float mb = rbf(mean);
    const float rden = __builtin_amdgcn_rcpf(rbf(__builtin_amdgcn_sqrtf(rbf(var + 1e-5f))));
    v4u o;
#pragma unroll
    for (int j = 0; j < 4; ++j) {
      const unsigned dpk = cvtpk(v[2 * j] - mb, v[2 * j + 1] - mb);
      o[j] = cvtpk(bf_lo(dpk) * rden, bf_hi(dpk) * rden);
    }
    if (g == part) *(LAS v4u*)(L + L_XN + wv * 288 + c * 16) = o;
  }
  STAMP(1);
  BAR();
  const unsigned tag = __builtin_amdgcn_readfirstlane(ep) + 1u;
  const bool up = c >= 8;

  {
    const v4u bx = *(LAS v4u*)(L + L_XN + (c & 7) * 288 + (wv & 3) * 64 + g * 16);
    f32x4 d[4];
#pragma unroll
    for (int dd = 0; dd < 4; ++dd) {
      v4u a;
#pragma unroll
      for (int jj = 0; jj < 4; ++jj) a[jj] = (dd & 1) ? phi(fw[2 * jj + 1][dd >> 1], fw[2 * jj][dd >> 1]) : plo(fw[2 * jj + 1][dd >> 1], fw[2 * jj][dd >> 1]);
      d[dd] = mfma(a, bx, f32x4{0.f, 0.f, 0.f, 0.f});
    }
#pragma unroll
    for (int rr = 0; rr < 2; ++rr) {
      f32x4 o;
#pragma unroll
      for (int dd = 0; dd < 4; ++dd) o[dd] = up ? d[dd][rr + 2] : d[dd][rr];
      *(LAS f32x4*)(L + L_RED + (wv * 8 + (c & 7)) * 272 + g * 64 + (rr + (up ? 2 : 0)) * 16) = o;
    }
  }
  STAMP(2);
  BAR();
  const rsrc_t rfr = mkrsrc(ws, front ? WS_END : 0);
  {
    const int t = tid & 255, b = t >> 5, pr = t & 31;
    const int ra = L_RED + ((wv & 4) * 8 + b) * 272 + (wv < 4 ? pr * 8 : pr * 4), rb = ra + (wv < 4 ? 4 : 128);
    float a0 = 0.f, a1 = 0.f;
#pragma unroll
    for (int k = 0; k < 4; ++k) { a0 += *(LAS float*)(L + ra + k * 8 * 272); a1 += *(LAS float*)(L + rb + k * 8 * 272); }
    const int off = wv < 4 ? WS_QP + ((xcd * 8 + b) * 4 + part) * 4096 + grp * 512 + pr * 16 : WS_GU + (fr * 4 + part) * 4096 + t * 16;
    __builtin_amdgcn_raw_buffer_store_b128(unit(a0, a1, tag), rfr, off, 0, AUX_SC1);
  }
  STAMP(3);
  SB();

  LAS unsigned char* Kw = L + L_K + wv * 8192;
#pragma unroll
  for (int t = 0; t < 4; ++t) *(LAS v4u*)(Kw + (4 * t + g) * 256 + ((c ^ (4 * t + g)) * 16)) = kv[t];
  SB();
  STAMP(4);
  const int goff = (wv < 4 ? WS_QP + (xcd * 8 + grp) * 16384 : WS_GU + fr * 16384) + (tid & 255) * 16;
  v4u pu[4];
#pragma unroll
  for (int k = 0; k < 4; ++k) pu[k] = __builtin_amdgcn_raw_buffer_load_b128(rfr, goff, k * 4096, AUX_SC1);
  SB();
  v4u vv[8];
#pragma unroll
  for (int t = 0; t < 4; ++t) vv[t] = __builtin_amdgcn_raw_buffer_load_b128(rv, wv * 8192 + g * 1024 + c * 16 + t * 256, 0, AUX_STREAM);
  SB();
#pragma unroll
  for (int t = 4; t < 8; ++t) *(LAS v4u*)(Kw + (4 * t + g) * 256 + ((c ^ ((4 * t + g) & 15)) * 16)) = kv[t];
  SB();
#pragma unroll
  for (int t = 4; t < 8; ++t) vv[t] = __builtin_amdgcn_raw_buffer_load_b128(rv, wv * 8192 + g * 1024 + c * 16 + (t - 4) * 256, 4096, AUX_STREAM);
  SB();
  STAMP(5);

  {
    bool bad = front & !(uok(pu[0], tag) & uok(pu[1], tag) & uok(pu[2], tag) & uok(pu[3], tag));
    if (__builtin_expect(__any(bad), 0)) {
      unsigned spin = 0;
      do {
        __builtin_amdgcn_s_sleep(POLL_SLEEP);
        if (bad) repoll4(pu[0], pu[1], pu[2], pu[3], rws, goff, goff + 4096, goff + 8192, goff + 12288);
        bad = front & !(uok(pu[0], tag) & uok(pu[1], tag) & uok(pu[2], tag) & uok(pu[3], tag));
      } while (__any(bad) && ++spin < SPIN_CAP);
      nsp0 = spin + 1;
    }
    const float a0 = (__uint_as_float(pu[0].x) + __uint_as_float(pu[1].x)) + (__uint_as_float(pu[2].x) + __uint_as_float(pu[3].x));
    const float a1 = (__uint_as_float(pu[0].z) + __uint_as_float(pu[1].z)) + (__uint_as_float(pu[2].z) + __uint_as_float(pu[3].z));
    const unsigned pk = cvtpk(a0, a1);
    if (wv < 4) {
      *(LAS unsigned*)(L + L_Q + wv * 288 + lane * 4) = pk;
    } else {
      const float sg = rbf(silu(bf_lo(pk)));
      const unsigned hb = cvtpk(sg * bf_hi(pk), 0.f);
      const int t = tid & 255;
      *(LAS u16*)(L + L_H + (t >> 5) * 96 + (t & 31) * 2) = (u16)hb;
    }
  }
  STAMP(6);
  BAR();

  constexpr float C2 = 0.08838834764831845f * 1.4426950408889634f;
  f32x4 s0 = {0.f, 0.f, 0.f, 0.f}, s1 = {0.f, 0.f, 0.f, 0.f};
#pragma unroll
  for (int i = 0; i < 4; ++i) {
    const v4u bq = *(LAS v4u*)(L + L_Q + (c >> 2) * 288 + i * 64 + g * 16);
    const v4u a0 = *(LAS v4u*)(Kw + c * 256 + (((4 * i + g) ^ c) * 16));
    const v4u a1 = *(LAS v4u*)(Kw + 4096 + c * 256 + (((4 * i + g) ^ c) * 16));
    s0 = mfma(a0, bq, s0);
    s1 = mfma(a1, bq, s1);
  }
  {
    float mx = fmaxf(fmaxf(fmaxf(s0[0], s0[1]), fmaxf(s0[2], s0[3])), fmaxf(fmaxf(s1[0], s1[1]), fmaxf(s1[2], s1[3])));
    mx = xrow_max(mx);
    if ((lane & 0x33) == 0) *(LAS float*)(L + L_M + (c >> 2) * 32 + wv * 4) = mx;
  }
  BAR();
  v4u pa;
  {
    const f32x4 m0 = *(LAS f32x4*)(L + L_M + (c >> 2) * 32), m1 = *(LAS f32x4*)(L + L_M + (c >> 2) * 32 + 16);
    const float M = fmaxf(fmaxf(fmaxf(m0[0], m0[1]), fmaxf(m0[2], m0[3])), fmaxf(fmaxf(m1[0], m1[1]), fmaxf(m1[2], m1[3])));
    const float mc = M * C2;
    float p[8];
#pragma unroll
    for (int r = 0; r < 4; ++r) { p[r] = __builtin_amdgcn_exp2f(fmaf(s0[r], C2, -mc)); p[4 + r] = __builtin_amdgcn_exp2f(fmaf(s1[r], C2, -mc)); }
    float ls = ((p[0] + p[1]) + (p[2] + p[3])) + ((p[4] + p[5]) + (p[6] + p[7]));
    ls = xrow_sum(ls);
    if ((lane & 0x33) == 0) *(LAS float*)(L + L_L + (c >> 2) * 32 + wv * 4) = ls;
#pragma unroll
    for (int j = 0; j < 4; ++j) pa[j] = cvtpk(p[2 * j], p[2 * j + 1]);
  }
  STAMP(7);
  SB();
  const rsrc_t rwo = mkrsrc(wo + (size_t)ri * 64 * DM + part * 128, 64 * DM * 2);
  const rsrc_t rwd = mkrsrc(wd + (size_t)(ffn ? fr : 0) * 32 * DM + part * 128, ffn ? 32 * DM * 2 : 0);
  v4u bw[3];
  bw[0] = __builtin_amdgcn_raw_buffer_load_b128(rwd, (4 * wv + g) * 1024 + c * 16, 0, AUX_STREAM);
  bw[1] = __builtin_amdgcn_raw_buffer_load_b128(rwo, (8 * wv + g) * 1024 + c * 16, 0, AUX_STREAM);
  bw[2] = __builtin_amdgcn_raw_buffer_load_b128(rwo, (8 * wv + g) * 1024 + c * 16, 4096, AUX_STREAM);
  SB();

  {
    float o[8];
#pragma unroll
    for (int dd = 0; dd < 8; ++dd) {
      v4u b;
#pragma unroll
      for (int jj = 0; jj < 4; ++jj) b[jj] = (dd & 1) ? phi(vv[2 * jj + 1][dd >> 1], vv[2 * jj][dd >> 1]) : plo(vv[2 * jj + 1][dd >> 1], vv[2 * jj][dd >> 1]);
      const f32x4 r = mfma(pa, b, f32x4{0.f, 0.f, 0.f, 0.f});
      o[dd] = r[0];
    }
    *(LAS f32x4*)(L + L_RED + wv * 2048 + g * 256 + c * 16) = f32x4{o[0], o[1], o[2], o[3]};
    *(LAS f32x4*)(L + L_RED + wv * 2048 + 1024 + g * 256 + c * 16) = f32x4{o[4], o[5], o[6], o[7]};
  }
  STAMP(8);
  BAR();
  {
    const int plane = lane >> 5, cc = (lane >> 1) & 15, lo = lane & 1;
    const int ra = wv < 4 ? L_RED + plane * 1024 + wv * 256 + cc * 16 + lo * 8 : L_M + (wv - 4) * 32;
    float a0, a1;
    if (wv < 4) {
      a0 = 0.f; a1 = 0.f;
#pragma unroll
      for (int k = 0; k < 8; ++k) { const f32x2 v = *(LAS f32x2*)(L + ra + k * 2048); a0 += v.x; a1 += v.y; }
    } else {
      const f32x4 m0 = *(LAS f32x4*)(L + ra), m1 = *(LAS f32x4*)(L + ra + 16);
      const f32x4 l0 = *(LAS f32x4*)(L + ra + 128), l1 = *(LAS f32x4*)(L + ra + 144);
      a0 = fmaxf(fmaxf(fmaxf(m0[0], m0[1]), fmaxf(m0[2], m0[3])), fmaxf(fmaxf(m1[0], m1[1]), fmaxf(m1[2], m1[3]))) * C2;
      a1 = ((l0[0] + l0[1]) + (l0[2] + l0[3])) + ((l1[0] + l1[1]) + (l1[2] + l1[3]));
    }
    const int off = wv < 4 ? WS_OP + ((((xcd * 4 + wv) * 2 + (cc >> 3)) * 8 + grp) * 4 + part) * 512 + ((cc & 7) * 8 + plane * 4 + lo * 2) * 8
                           : (lane == 0 ? WS_ML + (((xcd * 4 + wv - 4) * 8 + grp) * 4 + part) * 16 : WS_END);
    __builtin_amdgcn_raw_buffer_store_b128(unit(a0, a1, tag), rws, off, 0, AUX_SC1);
  }
  STAMP(9);
  SB();

  const int obase = WS_OP + ((ri * 8 + wv) * 4 + g) * 512 + c * 16;
  const int mlbase = WS_ML + (((ri >> 1) * 8 + wv) * 4 + g) * 16;
  v4u ou0 = __builtin_amdgcn_raw_buffer_load_b128(rws, obase, 0, AUX_SC1);
  v4u ou1 = __builtin_amdgcn_raw_buffer_load_b128(rws, obase + 256, 0, AUX_SC1);
  v4u mlu = __builtin_amdgcn_raw_buffer_load_b128(rws, mlbase, 0, AUX_SC1);
  SB();
  *(LAS v4u*)(L + L_K + offb(64 + 4 * wv + g, c)) = bw[0];
  *(LAS v4u*)(L + L_K + offb(8 * wv + g, c)) = bw[1];
  *(LAS v4u*)(L + L_K + offb(8 * wv + 4 + g, c)) = bw[2];
  STAMP(10);
  BAR();
  v4u af[3];
#pragma unroll
  for (int ks = 0; ks < 3; ++ks) {
    const v2u lo = trread(L + L_K + offb(32 * ks + 8 * g + (c >> 2), 2 * wv + ((c & 3) >> 1)) + 8 * (c & 1));
    const v2u hi = trread(L + L_K + offb(32 * ks + 8 * g + 4 + (c >> 2), 2 * wv + ((c & 3) >> 1)) + 8 * (c & 1));
    af[ks] = v4u{lo.x, lo.y, hi.x, hi.y};
  }
  const int sfoff = WS_SF + ((c & 7) * 32 + part * 8 + wv) * 15360 + (2 * g + (up ? 1 : 0)) * 16;
  {
    const v4u bh = *(LAS v4u*)(L + L_H + (c & 7) * 96 + g * 16);
    const f32x4 d = mfma(af[2], bh, f32x4{0.f, 0.f, 0.f, 0.f});
    __builtin_amdgcn_raw_buffer_store_b128(unit(up ? d[2] : d[0], up ? d[3] : d[1], tag), rws, ffn ? sfoff + (64 + fr) * 128 : WS_END, 0, AUX_SC1);
  }
  SB();
  {
    bool bad = !(uok(ou0, tag) & uok(ou1, tag) & uok(mlu, tag));
    if (__builtin_expect(__any(bad), 0)) {
      unsigned spin = 0;
      do {
        __builtin_amdgcn_s_sleep(POLL_SLEEP);
        if (bad) repoll3(ou0, ou1, mlu, rws, obase, mlbase);
        bad = !(uok(ou0, tag) & uok(ou1, tag) & uok(mlu, tag));
      } while (__any(bad) && ++spin < SPIN_CAP);
      nsp1 = spin + 1;
    }
  }
  STAMP(11);
  {
    const float ms = __uint_as_float(mlu.x), lsp = __uint_as_float(mlu.z);
    const float M = xrow_max(ms);
    const float e = __builtin_amdgcn_exp2f(ms - M);
    const float Lt = xrow_sum(e * lsp);
    const float wgt = e * __builtin_amdgcn_rcpf(Lt);
    const float o0 = xrow_sum(wgt * __uint_as_float(ou0.x)), o1 = xrow_sum(wgt * __uint_as_float(ou0.z));
    const float o2 = xrow_sum(wgt * __uint_as_float(ou1.x)), o3 = xrow_sum(wgt * __uint_as_float(ou1.z));
    const unsigned pk = cvtpk(g == 0 ? o0 : o2, g == 0 ? o1 : o3);
    if (g < 2) *(LAS unsigned*)(L + L_O16 + wv * 160 + g * 64 + c * 4) = pk;
  }
  BAR();
  {
    const v4u bo0 = *(LAS v4u*)(L + L_O16 + (c & 7) * 160 + g * 16), bo1 = *(LAS v4u*)(L + L_O16 + (c & 7) * 160 + 64 + g * 16);
    f32x4 d = mfma(af[0], bo0, f32x4{0.f, 0.f, 0.f, 0.f});
    d = mfma(af[1], bo1, d);
    __builtin_amdgcn_raw_buffer_store_b128(unit(up ? d[2] : d[0], up ? d[3] : d[1], tag), rws, sfoff + ri * 128, 0, AUX_SC1);
  }
  STAMP(12);
  SB();

  {
    const int line0 = wv < 4 ? 16 * wv : 64 + 14 * (wv - 4);
    const int fa = WS_SF + w * 15360 + line0 * 128 + lane * 16;
    const bool two = (wv < 4) | (lane < 48);
    const int fb = two ? fa + 1024 : fa;
    v4u f0 = __builtin_amdgcn_raw_buffer_load_b128(rws, fa, 0, AUX_SC1);
    v4u f1 = __builtin_amdgcn_raw_buffer_load_b128(rws, fb, 0, AUX_SC1);
    bool bad = !(uok(f0, tag) & uok(f1, tag));
    if (__any(bad)) {
      unsigned spin = 0;
      do {
        __builtin_amdgcn_s_sleep(POLL_SLEEP);
        if (bad) repoll2(f0, f1, rws, fa, fb);
        bad = !(uok(f0, tag) & uok(f1, tag));
      } while (__any(bad) && ++spin < SPIN_CAP);
      nsp2 = spin + 1;
    }
    STAMP(13);
    float a0 = __uint_as_float(f0.x) + (two ? __uint_as_float(f1.x) : 0.f);
    float a1 = __uint_as_float(f0.z) + (two ? __uint_as_float(f1.z) : 0.f);
    a0 += dpp<XOR8>(a0); a1 += dpp<XOR8>(a1);
    a0 = xrow_sum(a0); a1 = xrow_sum(a1);
    if (lane < 8) *(LAS f32x2*)(L + L_FIN + wv * 64 + lane * 8) = f32x2{a0, a1};
  }
  BAR();
  if (wv == 0 && lane < 16) {
    float s = 0.f, f = 0.f;
#pragma unroll
    for (int k = 0; k < 4; ++k) { s += *(LAS float*)(L + L_FIN + k * 64 + lane * 4); f += *(LAS float*)(L + L_FIN + (4 + k) * 64 + lane * 4); }
    const float gt = rbf(silu(rbf(f)));
    const float y = bf_lo(xfin) + rbf(rbf(s) * gt);
    out[w * 16 + lane] = (u16)cvtpk(y, y);
    if (w == 0 && lane == 0) __builtin_amdgcn_raw_buffer_store_b32(tag, rws, 0, WS_EPOCH, AUX_SC1);
  }
  STAMP(14);
  if (wv == 0) { const u64 d = st[LEAK] - st[0]; while (__builtin_amdgcn_s_memrealtime() - st[14] < d) __builtin_amdgcn_s_sleep(1); }
  if (tid == 12345) {
    u64* sp = (u64*)(ws + WS_STAMP) + w * 16;
#pragma unroll
    for (int i = 0; i < 15; ++i) sp[i] = st[i];
    sp[15] = (u64)nsp0 | ((u64)nsp1 << 20) | ((u64)nsp2 << 40);
  }
}

__global__ void __launch_bounds__(512) aux_kernel(unsigned* p) { __shared__ unsigned sm[512]; sm[threadIdx.x] = threadIdx.x; __syncthreads(); if (sm[(threadIdx.x + 1) % 512] == 99999u) p[0] = 1; }
extern "C" void kernel_launch(void* const* d_in, const int* in_sizes, int n_in, void* d_out, int out_size, void* d_ws, size_t ws_size,
                              hipStream_t stream) {
  fused_block<<<256, 512, 0, stream>>>((const u16*)d_in[0], (const u16*)d_in[1], (const u16*)d_in[2], (const u16*)d_in[3], (const u16*)d_in[4],
                                       (const u16*)d_in[6], (const u16*)d_in[7], (const u16*)d_in[8], (u16*)d_out, (unsigned char*)d_ws);
  aux_kernel<<<256, 512, 0, stream>>>((unsigned*)d_ws + (WS_STAMP / 4));
}
```

```cpp
#include <hip/hip_runtime.h>
#include <stdint.h>

#define AUX_STREAM 2
#define STAMPS 0
#define LWIN 8
#define QTRIG 16
#define DNPOS 32
#define FTRIG 36
#define OTRIG 44
#define ROTF ((((w * 5) >> 3) & 15) * 4096)
#define TOUCH 0
#define GU_EARLY 17
#define WS_BASE 0
#define POLL_SLEEP 1

typedef unsigned short u16;
typedef unsigned long long u64;
typedef unsigned v4u __attribute__((ext_vector_type(4)));
typedef unsigned v2u __attribute__((ext_vector_type(2)));
typedef float f32x4 __attribute__((ext_vector_type(4)));
typedef float f32x2 __attribute__((ext_vector_type(2)));
typedef short bf16x8 __attribute__((ext_vector_type(8)));
typedef short s16x4 __attribute__((ext_vector_type(4)));
typedef __amdgpu_buffer_rsrc_t rsrc_t;
#define LAS __attribute__((address_space(3)))

constexpr int NB = 8, DM = 512, HQ = 32, HKV = 8, SK = 4096, DH = 128, WIN = 1024, FF = 1792;
constexpr int AUX_SC1 = 16;
constexpr int RSRC_W3 = 0x00020000;
constexpr unsigned SPIN_CAP = 1u << 18;

constexpr int WS_EPOCH = 0;
constexpr int WS_ML = 4096;
constexpr int WS_QP = WS_ML + 256 * 128;
constexpr int WS_GU = WS_QP + 8 * 8 * 2 * 2048;
constexpr int WS_OP = WS_GU + 112 * 2048;
constexpr int WS_SF = WS_OP + 64 * 8 * 4 * 256;
constexpr int WS_END = WS_SF + 128 * 15360;
constexpr int WS_STAMP = 8 << 20;

constexpr int L_K = 0;
constexpr int L_V = 65536;
constexpr int L_WQ = L_V + 49152;
constexpr int L_BW = 131072;
constexpr int L_XN = L_K + 57344;
constexpr int L_Q = L_BW + 24576;
constexpr int L_H = L_Q + 4 * 288;
constexpr int L_P = L_H + 8 * 96;
constexpr int L_M = L_P + 4 * 544;
constexpr int L_L = L_M + 128;
constexpr int L_FLG = L_L + 128;
constexpr int L_END = L_FLG + 64;
constexpr int L_O16 = L_K;
constexpr int L_FIN = L_K + 2048;
static_assert(L_END <= 163840, "LDS map");
constexpr int F_XN = L_FLG, F_C1 = L_FLG + 4, F_Q = L_FLG + 12, F_P = L_FLG + 16, F_LD = L_FLG + 20, F_O16 = L_FLG + 24, F_FIN = L_FLG + 28,
              F_H = L_FLG + 32, F_F = L_FLG + 36, L_TAG = L_FLG + 48;

constexpr int NGRP = 46;
constexpr int kv_end(int i) { return 8 + i + 1 + (i >= DNPOS ? 2 : 0); }
constexpr int DN_END = 8 + DNPOS + 2;

__device__ __forceinline__ float bf_lo(unsigned u) { return __uint_as_float(u << 16); }
__device__ __forceinline__ float bf_hi(unsigned u) { return __uint_as_float(u & 0xffff0000u); }
__device__ __forceinline__ unsigned cvtpk(float lo, float hi) {
  typedef __bf16 bf16x2 __attribute__((ext_vector_type(2)));
  return __builtin_bit_cast(unsigned, __builtin_convertvector(f32x2{lo, hi}, bf16x2));
}
__device__ __forceinline__ float rbf(float f) { return bf_lo(cvtpk(f, f)); }
template <int CTRL> __device__ __forceinline__ float dpp(float x) {
  return __builtin_bit_cast(float, __builtin_amdgcn_mov_dpp(__builtin_bit_cast(int, x), CTRL, 0xf, 0xf, true));
}
constexpr int XOR1 = 0xB1, XOR2 = 0x4E, XOR7 = 0x141, XOR8 = 0x128;
__device__ __forceinline__ float xrow_sum(float x) {
  auto s = __builtin_amdgcn_permlane16_swap(__float_as_uint(x), __float_as_uint(x), false, false);
  x = __uint_as_float(s[0]) + __uint_as_float(s[1]);
  auto t = __builtin_amdgcn_permlane32_swap(__float_as_uint(x), __float_as_uint(x), false, false);
  return __uint_as_float(t[0]) + __uint_as_float(t[1]);
}
__device__ __forceinline__ float xrow_max(float x) {
  auto s = __builtin_amdgcn_permlane16_swap(__float_as_uint(x), __float_as_uint(x), false, false);
  x = fmaxf(__uint_as_float(s[0]), __uint_as_float(s[1]));
  auto t = __builtin_amdgcn_permlane32_swap(__float_as_uint(x), __float_as_uint(x), false, false);
  return fmaxf(__uint_as_float(t[0]), __uint_as_float(t[1]));
}
__device__ __forceinline__ float wave_sum(float x) {
  x += dpp<XOR1>(x); x += dpp<XOR2>(x); x += dpp<XOR7>(x); x += dpp<XOR8>(x);
  return xrow_sum(x);
}
__device__ __forceinline__ f32x4 mfma(v4u a, v4u b, f32x4 c) {
  return __builtin_amdgcn_mfma_f32_16x16x32_bf16(__builtin_bit_cast(bf16x8, a), __builtin_bit_cast(bf16x8, b), c, 0, 0, 0);
}
__device__ __forceinline__ unsigned plo(unsigned r1, unsigned r0) { return __builtin_amdgcn_perm(r1, r0, 0x05040100u); }
__device__ __forceinline__ unsigned phi(unsigned r1, unsigned r0) { return __builtin_amdgcn_perm(r1, r0, 0x07060302u); }
__device__ __forceinline__ rsrc_t mkrsrc(const void* p, unsigned bytes) { return __builtin_amdgcn_make_buffer_rsrc((void*)p, 0, bytes, RSRC_W3); }
__device__ __forceinline__ v4u unit(float a, float b, unsigned tag) { return v4u{__float_as_uint(a), tag, __float_as_uint(b), tag}; }
__device__ __forceinline__ bool uok(v4u u, unsigned tag) { return (u.y == tag) & (u.w == tag); }
__device__ __forceinline__ float silu(float x) { return x * __builtin_amdgcn_rcpf(1.f + __builtin_amdgcn_exp2f(-1.4426950408889634f * x)); }
__device__ __forceinline__ float max8(f32x4 a, f32x4 b) { return fmaxf(fmaxf(fmaxf(a[0], a[1]), fmaxf(a[2], a[3])), fmaxf(fmaxf(b[0], b[1]), fmaxf(b[2], b[3]))); }
#define SB() __builtin_amdgcn_sched_barrier(0)
template <typename T> __device__ __forceinline__ T ldnt(const T* p) { return AUX_STREAM == 2 ? __builtin_nontemporal_load(p) : *p; }
__device__ __forceinline__ v2u trread(LAS unsigned char* p) { return __builtin_bit_cast(v2u, __builtin_amdgcn_ds_read_tr16_b64_v4i16((LAS s16x4*)p)); }
__device__ __forceinline__ int offb(int row, int ch) { return 256 * row + 16 * (ch ^ (((row & 3) << 2) | ((row >> 2) & 3))); }
__device__ __forceinline__ v4u ldsc1(rsrc_t rs, int off) { return __builtin_amdgcn_raw_buffer_load_b128(rs, off, 0, AUX_SC1); }
__device__ __forceinline__ void stsc1(v4u v, rsrc_t rs, int off) { __builtin_amdgcn_raw_buffer_store_b128(v, rs, off, 0, AUX_SC1); }

__device__ __forceinline__ void sig(int flag, int lane) {
  if (lane == 0) asm volatile("ds_add_u32 %0, %1" :: "v"(flag), "v"(1u) : "memory");
}
__device__ __forceinline__ void wait_ge(int flag, unsigned n) {
  unsigned v;
  asm volatile("1:\n\tds_read_b32 %0, %1\n\ts_waitcnt lgkmcnt(0)\n\tv_cmp_gt_u32 vcc, %2, %0\n\ts_cbranch_vccnz 1b"
               : "=&v"(v) : "v"(flag), "s"(n) : "vcc", "memory");
}
#define G_HEAD "s_mov_b64 %[sv], exec\n\ts_mov_b32 %[cnt], 0x40000\n1:\n\t"
#define G_LD(r0, r3, o) "buffer_load_dwordx4 v[" #r0 ":" #r3 "], %[" #o "], %[rs], 0 offen sc1\n\t"
#define G_LDO(r0, r3, o, imm) "buffer_load_dwordx4 v[" #r0 ":" #r3 "], %[" #o "], %[rs], 0 offen offset:" #imm " sc1\n\t"
#define G_CK0(a, b) "s_waitcnt vmcnt(0)\n\tv_cmp_ne_u32 vcc, %[tag], v" #a "\n\tv_cmp_ne_u32 %[m], %[tag], v" #b "\n\ts_or_b64 vcc, vcc, %[m]\n\t"
#define G_CK(a, b) "v_cmp_ne_u32 %[m], %[tag], v" #a "\n\ts_or_b64 vcc, vcc, %[m]\n\tv_cmp_ne_u32 %[m], %[tag], v" #b "\n\ts_or_b64 vcc, vcc, %[m]\n\t"
#define G_STR2(x) #x
#define G_STR(x) G_STR2(x)
#define G_TAIL "s_and_b64 exec, exec, vcc\n\ts_cbranch_execz 2f\n\ts_sub_u32 %[cnt], %[cnt], 1\n\ts_sleep " G_STR(POLL_SLEEP) "\n\ts_cbranch_scc0 1b\n2:\n\ts_mov_b64 exec, %[sv]"
#define G_TMP [sv] "=&s"(sv), [m] "=&s"(m), [cnt] "=&s"(cnt)
__device__ __forceinline__ unsigned gather2(v4u& a, v4u& b, rsrc_t rs, int oa, int ob, unsigned tag) {
  u64 sv, m; unsigned cnt;
  asm volatile(G_HEAD G_LD(64, 67, oa) G_LD(68, 71, ob) G_CK0(65, 67) G_CK(69, 71) G_TAIL
               : "=&{v[64:67]}"(a), "=&{v[68:71]}"(b), G_TMP : [oa] "v"(oa), [ob] "v"(ob), [rs] "s"(rs), [tag] "s"(tag) : "vcc", "scc", "memory");
  return 0x40000u - cnt;
}
template <int D1, int D2>
__device__ __forceinline__ unsigned gather4(v4u& a, v4u& b, v4u& c, v4u& d, rsrc_t rs, int oa, int ob, unsigned tag) {
  u64 sv, m; unsigned cnt;
  asm volatile(G_HEAD G_LD(64, 67, oa) G_LDO(68, 71, oa, %[d1]) G_LD(72, 75, ob) G_LDO(76, 79, ob, %[d2])
               G_CK0(65, 67) G_CK(69, 71) G_CK(73, 75) G_CK(77, 79) G_TAIL
               : "=&{v[64:67]}"(a), "=&{v[68:71]}"(b), "=&{v[72:75]}"(c), "=&{v[76:79]}"(d), G_TMP
               : [oa] "v"(oa), [ob] "v"(ob), [rs] "s"(rs), [tag] "s"(tag), [d1] "i"(D1), [d2] "i"(D2) : "vcc", "scc", "memory");
  return 0x40000u - cnt;
}
__device__ __forceinline__ v4u unit4(f32x4 d, unsigned tag) { return v4u{cvtpk(d[0], d[1]), tag, cvtpk(d[2], d[3]), tag}; }
__device__ __forceinline__ f32x4 val4(v4u u) { return f32x4{bf_lo(u.x), bf_hi(u.x), bf_lo(u.z), bf_hi(u.z)}; }
__device__ __forceinline__ v4u trfrag(LAS unsigned char* p0, LAS unsigned char* p1) { const v2u lo = trread(p0), hi = trread(p1); return v4u{lo.x, lo.y, hi.x, hi.y}; }
__device__ __forceinline__ float xrow_sum4(f32x4 v) {
  auto s = __builtin_amdgcn_permlane16_swap(__float_as_uint(v[0]), __float_as_uint(v[1]), false, false);
  auto t = __builtin_amdgcn_permlane16_swap(__float_as_uint(v[2]), __float_as_uint(v[3]), false, false);
  const float x = __uint_as_float(s[0]) + __uint_as_float(s[1]), y = __uint_as_float(t[0]) + __uint_as_float(t[1]);
  auto u = __builtin_amdgcn_permlane32_swap(__float_as_uint(x), __float_as_uint(y), false, false);
  return __uint_as_float(u[0]) + __uint_as_float(u[1]);
}
__device__ __forceinline__ f32x4 ror8_sum(f32x4 v) { return f32x4{v[0] + dpp<XOR8>(v[0]), v[1] + dpp<XOR8>(v[1]), v[2] + dpp<XOR8>(v[2]), v[3] + dpp<XOR8>(v[3])}; }
#define DMA_POL " nt"
template <int LDSB, int SOFF>
__device__ __forceinline__ void dma_group(rsrc_t rs, int v0, int v1, int v2, int v3) {
  unsigned so;
  asm volatile("s_mov_b32 m0, %6\n\ts_mov_b32 %0, %7\n\t"
               "buffer_load_dwordx4 %1, %5, %0 offen" DMA_POL " lds\n\t"
               "buffer_load_dwordx4 %2, %5, %0 offen offset:1024" DMA_POL " lds\n\t"
               "buffer_load_dwordx4 %3, %5, %0 offen offset:2048" DMA_POL " lds\n\t"
               "buffer_load_dwordx4 %4, %5, %0 offen offset:3072" DMA_POL " lds"
               : "=&s"(so) : "v"(v0), "v"(v1), "v"(v2), "v"(v3), "s"(rs), "i"(LDSB), "i"(SOFF) : "memory");
}
template <int LDSB>
__device__ __forceinline__ void dma_group_s(rsrc_t rs, int so, int v0, int v1, int v2, int v3) {
  asm volatile("s_mov_b32 m0, %5\n\ts_nop 0\n\t"
               "buffer_load_dwordx4 %0, %4, %6 offen" DMA_POL " lds\n\t"
               "buffer_load_dwordx4 %1, %4, %6 offen offset:1024" DMA_POL " lds\n\t"
               "buffer_load_dwordx4 %2, %4, %6 offen offset:2048" DMA_POL " lds\n\t"
               "buffer_load_dwordx4 %3, %4, %6 offen offset:3072" DMA_POL " lds"
               :: "v"(v0), "v"(v1), "v"(v2), "v"(v3), "s"(rs), "i"(LDSB), "s"(so) : "memory");
}
template <int N> __device__ __forceinline__ void landed(int flagaddr, unsigned cnt) {
  asm volatile("s_waitcnt vmcnt(%2)\n\tds_write_b32 %0, %1" :: "v"(flagaddr), "v"(cnt), "i"(N) : "memory");
}

#define STAMP(i)

struct LoadCtx { rsrc_t rk, rv, rd, ro; int vk[4], vv[4], vb[4], rot; };
#define LST_ARG
#define LST_PASS
template <int N> __device__ __forceinline__ void loader_step(const LoadCtx& x LST_ARG) {
  if constexpr (N >= LWIN) landed<4 * (LWIN - 1)>(F_LD, N - LWIN + 1);
  if constexpr (N >= LWIN && (N - LWIN + 1) % 4 == 0) STAMP((N - LWIN + 1) / 4 + 1);
  constexpr int I = N - 8 - (N >= DN_END ? 2 : 0);
  if constexpr (I == 14) wait_ge(F_C1, 2);
  if constexpr (N >= DN_END - 2 && N < DN_END) dma_group<L_BW + 16384 + 4096 * (N - DN_END + 2), 16384 * (N - DN_END + 2)>(x.rd, x.vb[0], x.vb[1], x.vb[2], x.vb[3]);
  else if constexpr (I < 16) dma_group_s<L_K + 4096 * I>(x.rk, (x.rot + 4096 * I) & 0xffff, x.vk[0], x.vk[1], x.vk[2], x.vk[3]);
  else if constexpr (I < 32) {
    dma_group_s<L_V + 4096 * (I - 16)>(x.rv, (x.rot + 4096 * (I - 16)) & 0xffff, x.vv[0], x.vv[1], x.vv[2], x.vv[3]);
  } else dma_group<L_BW + 4096 * (N - 42), 16384 * (N - 42)>(x.ro, x.vb[0], x.vb[1], x.vb[2], x.vb[3]);
  if constexpr (N + 1 < NGRP) loader_step<N + 1>(x LST_PASS);
}
template <int R> __device__ __forceinline__ void loader_tail(int dummy LST_ARG) {
  landed<4 * (R - 1)>(F_LD, NGRP - R + 1);
  if constexpr ((NGRP - R + 1) % 4 == 0) STAMP((NGRP - R + 1) / 4 + 1);
  if constexpr (R > 1) loader_tail<R - 1>(dummy LST_PASS);
}

__global__ void __launch_bounds__(576, 3)
fused_block(const u16* __restrict__ x, const u16* __restrict__ kc, const u16* __restrict__ vc, const u16* __restrict__ wq,
            const u16* __restrict__ wo, const u16* __restrict__ wg, const u16* __restrict__ wu, const u16* __restrict__ wd,
            u16* __restrict__ out, unsigned char* ws) {
  __shared__ __attribute__((aligned(16))) unsigned char lds_raw[L_END];
  LAS unsigned char* L = (LAS unsigned char*)lds_raw;
#define NSP(i, x) (void)(x)
  STAMP(0);
  const int tid = threadIdx.x, lane = tid & 63, wv = __builtin_amdgcn_readfirstlane(tid >> 6);
  const int g = lane >> 4, c = lane & 15;
  const int w = blockIdx.x, xcd = w & 7, idx = w >> 3, part = idx & 3, grp = idx >> 2;
  const bool ffn = idx < 28;
  const int fr = xcd * 7 + grp;
  const int ri = xcd * 8 + grp;
  constexpr float C2 = 0.08838834764831845f * 1.4426950408889634f;
  const f32x4 z4 = {0.f, 0.f, 0.f, 0.f};
  asm volatile("" :: "s"(x), "s"(kc), "s"(vc), "s"(wq), "s"(wo), "s"(wg), "s"(wu), "s"(wd), "s"(out), "s"(ws));

  if (wv == 8) {
    {
      const int ft = xcd * 14 + idx - 16, rs = idx < 16 ? HQ * DH * 2 : FF * 2;
      const u16* fp = idx < 16 ? wq + xcd * 512 + (idx >> 1) * 64 : ((ft & 2) ? wu : wg) + (ft >> 2) * 64;
      const rsrc_t rq = mkrsrc((const unsigned char*)fp + (size_t)(idx & 1) * 256 * rs, idx < 30 ? 255 * rs + 128 : 0);
      int vq[4];
#pragma unroll
      for (int q = 0; q < 4; ++q) vq[q] = ((lane >> 3) + 8 * q) * rs - q * 1024 + (((lane & 7) ^ (((lane >> 3) & 2) | ((q & 1) << 2))) * 16);
      asm volatile("" :: "v"(vq[0]), "v"(vq[1]), "v"(vq[2]), "v"(vq[3]), "s"(rq));
      __builtin_amdgcn_s_barrier();
      STAMP(1);
      dma_group_s<L_WQ>(rq, 0, vq[0], vq[1], vq[2], vq[3]);
      dma_group_s<L_WQ + 4096>(rq, 32 * rs, vq[0], vq[1], vq[2], vq[3]);
      dma_group_s<L_WQ + 8192>(rq, 64 * rs, vq[0], vq[1], vq[2], vq[3]);
      dma_group_s<L_WQ + 12288>(rq, 96 * rs, vq[0], vq[1], vq[2], vq[3]);
      dma_group_s<L_WQ + 16384>(rq, 128 * rs, vq[0], vq[1], vq[2], vq[3]);
      dma_group_s<L_WQ + 20480>(rq, 160 * rs, vq[0], vq[1], vq[2], vq[3]);
      if constexpr (LWIN < 7) landed<4 * (LWIN - 1)>(F_LD, 7 - LWIN);
      dma_group_s<L_WQ + 24576>(rq, 192 * rs, vq[0], vq[1], vq[2], vq[3]);
      if constexpr (LWIN < 8) landed<4 * (LWIN - 1)>(F_LD, 8 - LWIN);
      dma_group_s<L_WQ + 28672>(rq, 224 * rs, vq[0], vq[1], vq[2], vq[3]);
    }
    SB();
    LoadCtx lc;
    const size_t kvoff = ((size_t)(grp * HKV + xcd) * SK + (SK - WIN) + part * 256) * DH;
    const int fro = ffn ? fr : 0;
    lc.rot = ROTF;
    lc.rk = mkrsrc(kc + kvoff, 65536);
    lc.rv = mkrsrc(vc + kvoff, 65536);
    lc.rd = mkrsrc(wd + (size_t)fro * 32 * DM + part * 128, ffn ? 31 * DM * 2 + 256 : 0);
    lc.ro = mkrsrc(wo + (size_t)ri * 64 * DM + part * 128, 63 * DM * 2 + 256);
#pragma unroll
    for (int q = 0; q < 4; ++q) {
      lc.vk[q] = g * 256 + ((c ^ (4 * q + g)) * 16);
      lc.vv[q] = g * 256 + ((c ^ ((g << 2) | q)) * 16);
      lc.vb[q] = g * 1024 + 3072 * q + ((c ^ ((g << 2) | q)) * 16);
    }
    loader_step<8>(lc LST_PASS);
    loader_tail<LWIN>(0 LST_PASS);
    STAMP(13);
  } else {
    if (wv == 0 && lane < 12) *(LAS unsigned*)(L + L_FLG + lane * 4) = 0u;
    const rsrc_t rx = mkrsrc(x, NB * DM * 2);
    const v4u xr = __builtin_amdgcn_raw_buffer_load_b128(rx, lane * 16, wv * 1024, 0);
    SB();
    const rsrc_t rws = mkrsrc(ws, WS_END);
    const unsigned ex = __builtin_amdgcn_raw_buffer_load_b32(wv == 0 ? rws : rx, wv == 0 ? WS_EPOCH : (wv == 4 ? ((w >> 4) * 8 + xcd) * 64 + c * 4 : NB * DM * 2), 0, AUX_SC1);
    asm volatile("s_waitcnt lgkmcnt(0)" ::: "memory");
    __builtin_amdgcn_s_barrier();
    asm volatile("" ::: "memory");
    SB();

    {
      float v[8];
#pragma unroll
      for (int j = 0; j < 4; ++j) { v[2 * j] = bf_lo(xr[j]); v[2 * j + 1] = bf_hi(xr[j]); }
      float s = ((v[0] + v[1]) + (v[2] + v[3])) + ((v[4] + v[5]) + (v[6] + v[7]));
      const float mean = wave_sum(s) * (1.f / DM);
      float q = 0.f;
#pragma unroll
      for (int j = 0; j < 8; ++j) { const float d = v[j] - mean; q = fmaf(d, d, q); }
      const float var = rbf(wave_sum(q) * (1.f / DM));
      const float mb = rbf(mean);
      const float rden = __builtin_amdgcn_rcpf(rbf(__builtin_amdgcn_sqrtf(rbf(var + 1e-5f))));
      v4u o;
#pragma unroll
      for (int j = 0; j < 4; ++j) {
        const unsigned dpk = cvtpk(v[2 * j] - mb, v[2 * j + 1] - mb);
        o[j] = cvtpk(bf_lo(dpk) * rden, bf_hi(dpk) * rden);
      }
      if ((g >> 1) == (idx & 1)) *(LAS v4u*)(L + L_XN + wv * 544 + (lane & 31) * 16) = o;
      if (wv == 0 && lane == 0) *(LAS unsigned*)(L + L_TAG) = ex + 1u;
    }
    sig(F_XN, lane);
    STAMP(1);
    wait_ge(F_XN, 8);
    const unsigned tag = __builtin_amdgcn_readfirstlane(*(LAS unsigned*)(L + L_TAG));
    const int q4 = c >> 2, p2 = (c & 3) >> 1, p8 = 8 * (c & 1);
    const bool up = c >= 8;
    if (wv < 2) {
      v4u bx[8];
#pragma unroll
      for (int ks = 0; ks < 8; ++ks) bx[ks] = *(LAS v4u*)(L + L_XN + (c & 7) * 544 + ks * 64 + g * 16);
      const int sw = (q4 & 2) | ((g & 1) << 2), ch = 4 * wv + p2;
      LAS unsigned char* a0 = L + L_WQ + (8 * g + q4) * 128 + p8 + ((ch ^ sw) * 16);
      LAS unsigned char* a1 = L + L_WQ + (8 * g + q4) * 128 + p8 + (((ch + 2) ^ sw) * 16);
      f32x4 d0 = z4, d1 = z4;
#pragma unroll
      for (int hh = 0; hh < 2; ++hh) {
        SB();
        wait_ge(F_LD, 4 + 4 * hh);
#pragma unroll
        for (int ks = 4 * hh; ks < 4 * hh + 4; ++ks) {
          d0 = mfma(trfrag(a0 + ks * 4096, a0 + ks * 4096 + 512), bx[ks], d0);
          d1 = mfma(trfrag(a1 + ks * 4096, a1 + ks * 4096 + 512), bx[ks], d1);
        }
      }
      STAMP(2);
      const int off = idx < 16 ? WS_QP + ((xcd * 8 + (c & 7)) * 2 + (idx & 1)) * 2048 + (idx >> 1) * 256
                               : (idx < 30 ? WS_GU + (xcd * 14 + idx - 16) * 2048 + (c & 7) * 256 : WS_END);
      stsc1(unit4(up ? d1 : d0, tag), rws, off + wv * 128 + (c >> 3) * 64 + g * 16);
      sig(F_C1, lane);
      STAMP(3);
    } else if (wv < 4) {
    } else if (wv < 6) {
      wait_ge(F_LD, QTRIG);
      STAMP(2);
      const int t = tid - 256, qo = WS_QP + (xcd * 8 + grp) * 4096 + t * 16;
      v4u u0, u1;
      NSP(0, gather2(u0, u1, rws, qo, qo + 2048, tag));
      const f32x4 q = val4(u0) + val4(u1);
      *(LAS v2u*)(L + L_Q + (t >> 5) * 288 + (t & 31) * 8) = v2u{cvtpk(q[0], q[1]), cvtpk(q[2], q[3])};
      sig(F_Q, lane);
      STAMP(3);
    }

      if (wv == 7 && ffn) {
        wait_ge(F_LD, GU_EARLY);
        const int go = WS_GU + (fr >> 1) * 8192 + (lane >> 3) * 256 + (fr & 1) * 128 + (lane & 7) * 16;
        v4u u[4];
        gather4<2048, 2048>(u[0], u[1], u[2], u[3], rws, go, go + 4096, tag);
        const f32x4 gg = val4(u[0]) + val4(u[1]), uu = val4(u[2]) + val4(u[3]);
        float h[4];
#pragma unroll
        for (int r = 0; r < 4; ++r) h[r] = rbf(silu(rbf(gg[r]))) * rbf(uu[r]);
        *(LAS v2u*)(L + L_H + (lane >> 3) * 96 + (lane & 7) * 8) = v2u{cvtpk(h[0], h[1]), cvtpk(h[2], h[3])};
        sig(F_H, lane);
      }
    wait_ge(F_LD, kv_end(15));
    STAMP(5);
    {
      v4u ka[8];
#pragma unroll
      for (int i = 0; i < 4; ++i) {
        ka[2 * i] = *(LAS v4u*)(L + L_K + wv * 8192 + c * 256 + (((4 * i + g) ^ c) * 16));
        ka[2 * i + 1] = *(LAS v4u*)(L + L_K + wv * 8192 + 4096 + c * 256 + (((4 * i + g) ^ c) * 16));
      }
      SB();
      wait_ge(F_Q, 2);
      STAMP(6);
      f32x4 s0 = z4, s1 = z4;
#pragma unroll
      for (int i = 0; i < 4; ++i) {
        const v4u bq = *(LAS v4u*)(L + L_Q + q4 * 288 + i * 64 + g * 16);
        s0 = mfma(ka[2 * i], bq, s0);
        s1 = mfma(ka[2 * i + 1], bq, s1);
      }
      const float mc = xrow_max(max8(s0, s1)) * C2;
      float p[8];
#pragma unroll
      for (int r = 0; r < 4; ++r) { p[r] = __builtin_amdgcn_exp2f(fmaf(s0[r], C2, -mc)); p[4 + r] = __builtin_amdgcn_exp2f(fmaf(s1[r], C2, -mc)); }
      const float ls = xrow_sum(((p[0] + p[1]) + (p[2] + p[3])) + ((p[4] + p[5]) + (p[6] + p[7])));
      const bool t1 = c & 1;
      const v2u pw = {cvtpk(t1 ? p[4] : p[0], t1 ? p[5] : p[1]), cvtpk(t1 ? p[6] : p[2], t1 ? p[7] : p[3])};
      if ((c & 2) == 0) *(LAS v2u*)(L + L_P + q4 * 544 + wv * 64 + (c & 1) * 32 + g * 8) = pw;
      if ((lane & 0x32) == 0) *(LAS float*)(L + L_M + (c & 1) * 128 + q4 * 32 + wv * 4) = t1 ? ls : mc;
    }
    sig(F_P, lane);
    STAMP(7);
    wait_ge(F_P, 8);

    if (wv < 4) {
      v4u pb[8];
#pragma unroll
      for (int ks = 0; ks < 8; ++ks) pb[ks] = *(LAS v4u*)(L + L_P + (c & 3) * 544 + ks * 64 + g * 16);
      float fk[8];
      {
        const f32x4 m0 = *(LAS f32x4*)(L + L_M + (c & 3) * 32), m1 = *(LAS f32x4*)(L + L_M + (c & 3) * 32 + 16);
        const f32x4 l0 = *(LAS f32x4*)(L + L_L + (c & 3) * 32), l1 = *(LAS f32x4*)(L + L_L + (c & 3) * 32 + 16);
        const float M = max8(m0, m1);
        float l = 0.f;
#pragma unroll
        for (int k = 0; k < 4; ++k) { fk[k] = __builtin_amdgcn_exp2f(m0[k] - M); fk[4 + k] = __builtin_amdgcn_exp2f(m1[k] - M); }
#pragma unroll
        for (int k = 0; k < 4; ++k) { l = fmaf(fk[k], l0[k], l); l = fmaf(fk[4 + k], l1[k], l); }
        if (wv == 0) stsc1(unit(M, l, tag), rws, lane < 4 ? WS_ML + ((xcd * 8 + grp) * 4 + part) * 128 + c * 16 : WS_END);
      }
      const int sfoff = WS_SF + ((c & 7) * 16 + part * 4 + wv) * 15360 + (c >> 3) * 64 + g * 16;
      int wa[4];
#pragma unroll
      for (int k = 0; k < 4; ++k) wa[k] = offb(8 * g + 4 * (k & 1) + q4, 4 * wv + 2 * (k >> 1) + p2) + p8;
      STAMP(4);
      f32x4 oa = z4, ob = z4;
#pragma unroll
      for (int j = 0; j < 4; ++j) {
        SB();
        wait_ge(F_LD, kv_end(19 + 4 * j));
#pragma unroll
        for (int kk = 0; kk < 2; ++kk) {
          const int ks = 2 * j + kk;
          const f32x4 da = mfma(trfrag(L + L_V + wa[0] + ks * 8192, L + L_V + wa[1] + ks * 8192), pb[ks], z4);
          const f32x4 db = mfma(trfrag(L + L_V + wa[2] + ks * 8192, L + L_V + wa[3] + ks * 8192), pb[ks], z4);
          oa += fk[ks] * da;
          ob += fk[ks] * db;
        }
      }
      stsc1(unit4((c & 4) ? ob : oa, tag), rws,
            c < 8 ? WS_OP + ((((xcd * 4 + (c & 3)) * 2 + (wv >> 1)) * 8 + grp) * 4 + part) * 256 + ((wv & 1) * 8 + (c & 4) + g) * 16 : WS_END);
      STAMP(8);
      SB();
      if (ffn) {
        wait_ge(F_H, 1);
        const v4u bh = *(LAS v4u*)(L + L_H + (c & 7) * 96 + g * 16);
        wait_ge(F_LD, DN_END);
        const f32x4 d0 = mfma(trfrag(L + L_BW + 16384 + wa[0], L + L_BW + 16384 + wa[1]), bh, z4);
        const f32x4 d1 = mfma(trfrag(L + L_BW + 16384 + wa[2], L + L_BW + 16384 + wa[3]), bh, z4);
        stsc1(unit4(up ? d1 : d0, tag), rws, sfoff + (64 + fr) * 128);
      }
      STAMP(9);
      SB();
      wait_ge(F_LD, NGRP);
      v4u af[4];
#pragma unroll
      for (int k = 0; k < 4; ++k) af[k] = trfrag(L + L_BW + wa[2 * (k >> 1)] + (k & 1) * 8192, L + L_BW + wa[2 * (k >> 1) + 1] + (k & 1) * 8192);
      STAMP(10);
      SB();
      wait_ge(F_O16, 4);
      STAMP(11);
      {
        const v4u bo0 = *(LAS v4u*)(L + L_O16 + (c & 7) * 160 + g * 16), bo1 = *(LAS v4u*)(L + L_O16 + (c & 7) * 160 + 64 + g * 16);
        const f32x4 d0 = mfma(af[1], bo1, mfma(af[0], bo0, z4));
        const f32x4 d1 = mfma(af[3], bo1, mfma(af[2], bo0, z4));
        stsc1(unit4(up ? d1 : d0, tag), rws, sfoff + ri * 128);
      }
      STAMP(12);
    } else {
      const int wb = wv - 4;
      const bool fin = (idx & 1) == 0;
      const int fbase = WS_SF + ((w >> 4) * 8 + xcd) * 15360 + lane * 16;
      wait_ge(F_LD, OTRIG);
      STAMP(8);
      {
        v4u ou[2], ml[2];
        NSP(1, (gather4<1024, 512>(ou[0], ou[1], ml[0], ml[1], rws, WS_OP + (ri * 8 + 2 * wb) * 1024 + lane * 16,
                                   WS_ML + ((xcd * 8 + 2 * wb) * 4 + g) * 128 + (grp >> 1) * 16, tag)));
        STAMP(10);
        float o[2];
#pragma unroll
        for (int k = 0; k < 2; ++k) {
          const float ms = __uint_as_float(ml[k].x), lsp = __uint_as_float(ml[k].z);
          const float e = __builtin_amdgcn_exp2f(ms - xrow_max(ms));
          o[k] = xrow_sum4(e * val4(ou[k])) * __builtin_amdgcn_rcpf(xrow_sum(e * lsp));
        }
        const unsigned pk = cvtpk(o[0], o[1]);
        *(LAS u16*)(L + L_O16 + (2 * wb) * 160 + c * 8 + g * 2) = (u16)pk;
        *(LAS u16*)(L + L_O16 + (2 * wb + 1) * 160 + c * 8 + g * 2) = (u16)(pk >> 16);
      }
      sig(F_O16, lane);
      STAMP(11);
      if (fin) {
        const int fo = fbase + (64 + 14 * wb) * 128;
        const bool two = lane < 48;
        v4u f0, f1;
        gather2(f0, f1, rws, fo, two ? fo + 1024 : fo, tag);
        const float f = xrow_sum4(ror8_sum(val4(f0) + (two ? val4(f1) : z4)));
        if (c < 8) *(LAS float*)(L + L_FIN + wb * 128 + c * 16 + g * 4) = f;
        sig(F_F, lane);
      }
      STAMP(9);
      float gt = 0.f;
      if (fin && wv == 4) {
        wait_ge(F_F, 4);
        float f = 0.f;
#pragma unroll
        for (int k = 0; k < 4; ++k) f += *(LAS float*)(L + L_FIN + k * 128 + (lane & 31) * 4);
        gt = rbf(silu(rbf(f)));
      }
      if (fin) {
        v4u s0, s1;
        NSP(2, gather2(s0, s1, rws, fbase + wb * 2048, fbase + wb * 2048 + 1024, tag));
        STAMP(12);
        const float s = xrow_sum4(ror8_sum(val4(s0) + val4(s1)));
        if (c < 8) *(LAS float*)(L + L_FIN + 512 + wb * 128 + c * 16 + g * 4) = s;
        sig(F_FIN, lane);
        if (wv == 4) {
          const unsigned xp = __builtin_amdgcn_ds_bpermute((lane & 30) * 2, ex);
          wait_ge(F_FIN, 4);
          float st_ = 0.f;
#pragma unroll
          for (int k = 0; k < 4; ++k) st_ += *(LAS float*)(L + L_FIN + 512 + k * 128 + (lane & 31) * 4);
          const float y = ((lane & 1) ? bf_hi(xp) : bf_lo(xp)) + rbf(rbf(st_) * gt);
          if (lane < 32) out[((w >> 4) * 8 + xcd) * 32 + lane] = (u16)cvtpk(y, y);
          if (w == 0 && lane == 0) __builtin_amdgcn_raw_buffer_store_b32(tag, rws, 0, WS_EPOCH, AUX_SC1);
        }
      }
      STAMP(13);
    }
  }
}

extern "C" void kernel_launch(void* const* d_in, const int* in_sizes, int n_in, void* d_out, int out_size, void* d_ws, size_t ws_size,
                              hipStream_t stream) {
  fused_block<<<256, 576, 0, stream>>>((const u16*)d_in[0], (const u16*)d_in[1], (const u16*)d_in[2], (const u16*)d_in[3], (const u16*)d_in[4],
                                       (const u16*)d_in[6], (const u16*)d_in[7], (const u16*)d_in[8], (u16*)d_out, (unsigned char*)d_ws + WS_BASE);
}
```

```cpp
#include <hip/hip_runtime.h>
#include <stdint.h>

#define CW_SHIFT 4
#define QTRIG 20
#define OTRIG 46
#define POLL_SLEEP 1

typedef unsigned short u16;
typedef unsigned long long u64;
typedef unsigned v4u __attribute__((ext_vector_type(4)));
typedef unsigned v2u __attribute__((ext_vector_type(2)));
typedef float f32x4 __attribute__((ext_vector_type(4)));
typedef float f32x2 __attribute__((ext_vector_type(2)));
typedef short bf16x8 __attribute__((ext_vector_type(8)));
typedef short s16x4 __attribute__((ext_vector_type(4)));
typedef __amdgpu_buffer_rsrc_t rsrc_t;
#define LAS __attribute__((address_space(3)))

constexpr int NB = 8, DM = 512, HQ = 32, HKV = 8, SK = 4096, DH = 128, WIN = 1024, FF = 1792;
constexpr int AUX_SC1 = 16;
constexpr int RSRC_W3 = 0x00020000;

constexpr int WS_EPOCH = 0;
constexpr int WS_ML = 4096;
constexpr int WS_QP = WS_ML + 256 * 128;
constexpr int WS_GU = WS_QP + 8 * 8 * 2 * 2048;
constexpr int WS_OP = WS_GU + 112 * 2048;
constexpr int WS_SF = WS_OP + 64 * 8 * 4 * 256;
constexpr int SF_B = 92 * 128;
constexpr int WS_END = WS_SF + 128 * SF_B;

constexpr int L_K = 0;
constexpr int L_V = 65536;
constexpr int L_WQ = L_V + 49152;
constexpr int L_XN = L_V + 32768;
constexpr int L_BW = 131072;
constexpr int L_Q = L_BW + 24576;
constexpr int L_H = L_Q + 4 * 288;
constexpr int L_P = L_H + 8 * 160;
constexpr int L_M = L_P + 4 * 544;
constexpr int L_L = L_M + 128;
constexpr int L_FLG = L_L + 128;
constexpr int L_END = L_FLG + 64;
constexpr int L_X = L_K + 57344;
constexpr int L_O16 = L_K;
constexpr int L_FIN = L_K + 2048;
static_assert(L_END <= 163840, "LDS map");
constexpr int F_LD = L_FLG, F_XN = L_FLG + 4, F_C1 = L_FLG + 8, F_Q = L_FLG + 12, F_P = L_FLG + 16, F_O16 = L_FLG + 20, F_FIN = L_FLG + 24,
              F_H = L_FLG + 28, F_F = L_FLG + 32, L_TAG = L_FLG + 48;

constexpr int X_END = 2, WQ_END = 6, GU_END = 10, K_END = 26, DN_END = 44, NGRP = 48;

__device__ __forceinline__ float bf_lo(unsigned u) { return __uint_as_float(u << 16); }
__device__ __forceinline__ float bf_hi(unsigned u) { return __uint_as_float(u & 0xffff0000u); }
__device__ __forceinline__ unsigned cvtpk(float lo, float hi) {
  typedef __bf16 bf16x2 __attribute__((ext_vector_type(2)));
  return __builtin_bit_cast(unsigned, __builtin_convertvector(f32x2{lo, hi}, bf16x2));
}
__device__ __forceinline__ float rbf(float f) { return bf_lo(cvtpk(f, f)); }
template <int CTRL> __device__ __forceinline__ float dpp(float x) {
  return __builtin_bit_cast(float, __builtin_amdgcn_mov_dpp(__builtin_bit_cast(int, x), CTRL, 0xf, 0xf, true));
}
constexpr int XOR1 = 0xB1, XOR2 = 0x4E, XOR7 = 0x141, XOR8 = 0x128;
__device__ __forceinline__ float xrow_sum(float x) {
  auto s = __builtin_amdgcn_permlane16_swap(__float_as_uint(x), __float_as_uint(x), false, false);
  x = __uint_as_float(s[0]) + __uint_as_float(s[1]);
  auto t = __builtin_amdgcn_permlane32_swap(__float_as_uint(x), __float_as_uint(x), false, false);
  return __uint_as_float(t[0]) + __uint_as_float(t[1]);
}
__device__ __forceinline__ float fmax_raw(float a, float b) { float r; asm("v_max_f32 %0, %1, %2" : "=v"(r) : "v"(a), "v"(b)); return r; }
__device__ __forceinline__ float xrow_max(float x) {
  auto s = __builtin_amdgcn_permlane16_swap(__float_as_uint(x), __float_as_uint(x), false, false);
  x = fmaxf(__uint_as_float(s[0]), __uint_as_float(s[1]));
  auto t = __builtin_amdgcn_permlane32_swap(__float_as_uint(x), __float_as_uint(x), false, false);
  return fmaxf(__uint_as_float(t[0]), __uint_as_float(t[1]));
}
__device__ __forceinline__ float xrow_sum4(f32x4 v) {
  auto s = __builtin_amdgcn_permlane16_swap(__float_as_uint(v[0]), __float_as_uint(v[1]), false, false);
  auto t = __builtin_amdgcn_permlane16_swap(__float_as_uint(v[2]), __float_as_uint(v[3]), false, false);
  const float x = __uint_as_float(s[0]) + __uint_as_float(s[1]), y = __uint_as_float(t[0]) + __uint_as_float(t[1]);
  auto u = __builtin_amdgcn_permlane32_swap(__float_as_uint(x), __float_as_uint(y), false, false);
  return __uint_as_float(u[0]) + __uint_as_float(u[1]);
}
__device__ __forceinline__ f32x4 ror8_sum(f32x4 v) { return f32x4{v[0] + dpp<XOR8>(v[0]), v[1] + dpp<XOR8>(v[1]), v[2] + dpp<XOR8>(v[2]), v[3] + dpp<XOR8>(v[3])}; }
__device__ __forceinline__ float wave_sum(float x) {
  x += dpp<XOR1>(x); x += dpp<XOR2>(x); x += dpp<XOR7>(x); x += dpp<XOR8>(x);
  return xrow_sum(x);
}
__device__ __forceinline__ f32x4 mfma(v4u a, v4u b, f32x4 c) {
  return __builtin_amdgcn_mfma_f32_16x16x32_bf16(__builtin_bit_cast(bf16x8, a), __builtin_bit_cast(bf16x8, b), c, 0, 0, 0);
}
__device__ __forceinline__ rsrc_t mkrsrc(const void* p, unsigned bytes) { return __builtin_amdgcn_make_buffer_rsrc((void*)p, 0, bytes, RSRC_W3); }
__device__ __forceinline__ float silu(float x) { return x * __builtin_amdgcn_rcpf(1.f + __builtin_amdgcn_exp2f(-1.4426950408889634f * x)); }
__device__ __forceinline__ float max8(f32x4 a, f32x4 b) { return fmaxf(fmaxf(fmaxf(a[0], a[1]), fmaxf(a[2], a[3])), fmaxf(fmaxf(b[0], b[1]), fmaxf(b[2], b[3]))); }
#define SB() __builtin_amdgcn_sched_barrier(0)
__device__ __forceinline__ int sel(bool p, int a, int b) { asm("" : "+v"(a)); asm("" : "+v"(b)); return p ? a : b; }
#define PIN(x) asm volatile("" : "+v"(x))
__device__ __forceinline__ v2u trread(LAS unsigned char* p) { return __builtin_bit_cast(v2u, __builtin_amdgcn_ds_read_tr16_b64_v4i16((LAS s16x4*)p)); }
__device__ __forceinline__ v4u trfrag(LAS unsigned char* p0, LAS unsigned char* p1) { const v2u lo = trread(p0), hi = trread(p1); return v4u{lo.x, lo.y, hi.x, hi.y}; }
__device__ __forceinline__ int offb(int row, int ch) { return 256 * row + 16 * (ch ^ (((row & 3) << 2) | ((row >> 2) & 3))); }
__device__ __forceinline__ void stsc1(v4u v, rsrc_t rs, int off) { __builtin_amdgcn_raw_buffer_store_b128(v, rs, off, 0, AUX_SC1); }
__device__ __forceinline__ v4u unit4(f32x4 d, unsigned tag) { return v4u{cvtpk(d[0], d[1]), tag, cvtpk(d[2], d[3]), tag}; }
__device__ __forceinline__ f32x4 val4(v4u u) { return f32x4{bf_lo(u.x), bf_hi(u.x), bf_lo(u.z), bf_hi(u.z)}; }
__device__ __forceinline__ int uni(int x) { asm volatile("" : "+s"(x)); return x; }

#define LDS_MASKED(OP, MASK, ADDR, VAL) do { u64 sv_; \
  asm volatile("s_mov_b64 %0, exec\n\ts_and_b64 exec, exec, %1\n\t" OP " %2, %3\n\ts_mov_b64 exec, %0" : "=&s"(sv_) : "s"((u64)(MASK)), "v"(ADDR), "v"(VAL) : "scc", "memory"); } while (0)
__device__ __forceinline__ void sig(int flag) { LDS_MASKED("ds_add_u32", 1, flag, 1u); }
__device__ __forceinline__ void wait_ge(int flag, unsigned n) {
  unsigned v;
  asm volatile("1:\n\tds_read_b32 %0, %1\n\ts_waitcnt lgkmcnt(0)\n\tv_cmp_gt_u32 vcc, %2, %0\n\ts_cbranch_vccnz 1b"
               : "=&v"(v) : "v"(flag), "s"(n) : "vcc", "memory");
}
#define WAIT_READ(READS) "1:\n\tds_read_b32 %[v], %[f]\n\t" READS "s_waitcnt lgkmcnt(0)\n\tv_cmp_gt_u32 vcc, %[n], %[v]\n\ts_cbranch_vccnz 1b"
__device__ __forceinline__ void wait_ge_b128x2(int flag, unsigned n, int a, v4u& x, v4u& y) {
  unsigned v;
  asm volatile(WAIT_READ("ds_read_b128 %[x], %[a]\n\tds_read_b128 %[y], %[a] offset:64\n\t")
               : [v] "=&v"(v), [x] "=&v"(x), [y] "=&v"(y) : [f] "v"(flag), [n] "s"(n), [a] "v"(a) : "vcc", "memory");
}
__device__ __forceinline__ float wait_ge_sum4(int flag, unsigned n, int a) {
  unsigned v; f32x2 x, y;
  asm volatile(WAIT_READ("ds_read2_b32 %[x], %[a] offset1:32\n\tds_read2_b32 %[y], %[a] offset0:64 offset1:96\n\t")
               : [v] "=&v"(v), [x] "=&v"(x), [y] "=&v"(y) : [f] "v"(flag), [n] "s"(n), [a] "v"(a) : "vcc", "memory");
  return (x[0] + x[1]) + (y[0] + y[1]);
}
template <int OFF> __device__ __forceinline__ void wait_ge_tr4(int flag, unsigned n, const int (&a)[4], v4u& x, v4u& y) {
  unsigned v; v2u r0, r1, r2, r3;
  asm volatile(WAIT_READ("ds_read_b64_tr_b16 %[r0], %[a0] offset:%[o]\n\tds_read_b64_tr_b16 %[r1], %[a1] offset:%[o]\n\t"
                         "ds_read_b64_tr_b16 %[r2], %[a2] offset:%[o]\n\tds_read_b64_tr_b16 %[r3], %[a3] offset:%[o]\n\t")
               : [v] "=&v"(v), [r0] "=&v"(r0), [r1] "=&v"(r1), [r2] "=&v"(r2), [r3] "=&v"(r3)
               : [f] "v"(flag), [n] "s"(n), [a0] "v"(a[0]), [a1] "v"(a[1]), [a2] "v"(a[2]), [a3] "v"(a[3]), [o] "i"(OFF) : "vcc", "memory");
  x = v4u{r0.x, r0.y, r1.x, r1.y}; y = v4u{r2.x, r2.y, r3.x, r3.y};
}
#define G_HEAD "s_mov_b64 %[sv], exec\n\ts_mov_b32 %[cnt], 0x40000\n1:\n\t"
#define G_LD(r0, r3, o) "buffer_load_dwordx4 v[" #r0 ":" #r3 "], %[" #o "], %[rs], 0 offen sc1\n\t"
#define G_LDO(r0, r3, o, imm) "buffer_load_dwordx4 v[" #r0 ":" #r3 "], %[" #o "], %[rs], 0 offen offset:" #imm " sc1\n\t"
#define G_X(t, a, b) "v_bitop3_b32 %[" #t "], v" #a ", v" #b ", %[tag] bitop3:0x7e\n\t"
#define G_W "s_waitcnt vmcnt(0)\n\t"
#define G_STR2(x) #x
#define G_STR(x) G_STR2(x)
#define G_TAIL "v_cmp_ne_u32 vcc, 0, %[t0]\n\ts_and_b64 exec, exec, vcc\n\ts_cbranch_execz 2f\n\ts_sub_u32 %[cnt], %[cnt], 1\n\ts_sleep " G_STR(POLL_SLEEP) "\n\ts_cbranch_scc0 1b\n2:\n\ts_mov_b64 exec, %[sv]"
#define G_TMP [sv] "=&s"(sv), [cnt] "=&s"(cnt), [t0] "=&v"(t0), [t1] "=&v"(t1), [t2] "=&v"(t2)
__device__ __forceinline__ void gather2(v4u& a, v4u& b, rsrc_t rs, int oa, int ob, unsigned tag) {
  u64 sv; unsigned cnt, t0, t1, t2;
  asm volatile(G_HEAD G_LD(64, 67, oa) G_LD(68, 71, ob) G_W G_X(t0, 65, 67) G_X(t1, 69, 71) "v_or_b32 %[t0], %[t0], %[t1]\n\t" G_TAIL
               : "=&{v[64:67]}"(a), "=&{v[68:71]}"(b), G_TMP : [oa] "v"(oa), [ob] "v"(ob), [rs] "s"(rs), [tag] "s"(tag) : "vcc", "scc", "memory");
}
__device__ __forceinline__ void gather1(v4u& a, rsrc_t rs, int oa, unsigned tag) {
  u64 sv; unsigned cnt, t0, t1, t2;
  asm volatile(G_HEAD G_LD(64, 67, oa) G_W G_X(t0, 65, 67) G_TAIL
               : "=&{v[64:67]}"(a), G_TMP : [oa] "v"(oa), [rs] "s"(rs), [tag] "s"(tag) : "vcc", "scc", "memory");
}
template <int D1, int D2>
__device__ __forceinline__ void gather4(v4u& a, v4u& b, v4u& c, v4u& d, rsrc_t rs, int oa, int ob, unsigned tag) {
  u64 sv; unsigned cnt, t0, t1, t2;
  asm volatile(G_HEAD G_LD(64, 67, oa) G_LDO(68, 71, oa, %[d1]) G_LD(72, 75, ob) G_LDO(76, 79, ob, %[d2])
               G_W G_X(t0, 65, 67) G_X(t1, 69, 71) G_X(t2, 73, 75) "v_or3_b32 %[t0], %[t0], %[t1], %[t2]\n\t" G_X(t1, 77, 79) "v_or_b32 %[t0], %[t0], %[t1]\n\t" G_TAIL
               : "=&{v[64:67]}"(a), "=&{v[68:71]}"(b), "=&{v[72:75]}"(c), "=&{v[76:79]}"(d), G_TMP
               : [oa] "v"(oa), [ob] "v"(ob), [rs] "s"(rs), [tag] "s"(tag), [d1] "i"(D1), [d2] "i"(D2) : "vcc", "scc", "memory");
}
#define LD_GROUP(rs, v, pol) \
  "buffer_load_dwordx4 %[" v "0], %[" rs "], %[so] offen" pol " lds\n\t" \
  "buffer_load_dwordx4 %[" v "1], %[" rs "], %[so] offen offset:1024" pol " lds\n\t" \
  "buffer_load_dwordx4 %[" v "2], %[" rs "], %[so] offen offset:2048" pol " lds\n\t" \
  "buffer_load_dwordx4 %[" v "3], %[" rs "], %[so] offen offset:3072" pol " lds\n\t"
#define LD_NEXT(step) "s_add_u32 m0, m0, 0x1000\n\ts_add_u32 %[so], %[so], " step "\n\t"
#define LD_LANDED(n) "s_waitcnt vmcnt(" #n ")\n\tds_write_b32 %[fld], %[cnt]\n\tv_add_u32 %[cnt], 1, %[cnt]\n\t"
#define LD_WAIT(lbl, off, n) lbl ":\n\tds_read_b32 %[t], %[fld] offset:" G_STR(off) "\n\ts_waitcnt lgkmcnt(0)\n\tv_cmp_gt_u32 vcc, " #n ", %[t]\n\ts_cbranch_vccnz " lbl "b\n\t"
#define V4(n, a) [n##0] "v"(a[0]), [n##1] "v"(a[1]), [n##2] "v"(a[2]), [n##3] "v"(a[3])

constexpr float C2 = 0.08838834764831845f * 1.4426950408889634f;

__device__ __forceinline__ void scores(LAS unsigned char* L, int wv, int g, int c) {
  const f32x4 z4 = {0.f, 0.f, 0.f, 0.f};
  wait_ge(F_LD, K_END);
  v4u ka[8];
#pragma unroll
  for (int i = 0; i < 4; ++i) {
    ka[2 * i] = *(LAS v4u*)(L + L_K + wv * 8192 + c * 256 + (((4 * i + g) ^ c) * 16));
    ka[2 * i + 1] = *(LAS v4u*)(L + L_K + wv * 8192 + 4096 + c * 256 + (((4 * i + g) ^ c) * 16));
  }
  SB();
  wait_ge(F_Q, 2);
  f32x4 s0 = z4, s1 = z4;
#pragma unroll
  for (int i = 0; i < 4; ++i) {
    const v4u bq = *(LAS v4u*)(L + L_Q + (c >> 2) * 288 + i * 64 + g * 16);
    s0 = mfma(ka[2 * i], bq, s0);
    s1 = mfma(ka[2 * i + 1], bq, s1);
  }
  const float mc = xrow_max(max8(s0, s1)) * C2;
  float p[8];
#pragma unroll
  for (int r = 0; r < 4; ++r) { p[r] = __builtin_amdgcn_exp2f(fmaf(s0[r], C2, -mc)); p[4 + r] = __builtin_amdgcn_exp2f(fmaf(s1[r], C2, -mc)); }
  const float ls = xrow_sum(((p[0] + p[1]) + (p[2] + p[3])) + ((p[4] + p[5]) + (p[6] + p[7])));
  const bool t1 = c & 1;
  const v2u pw = {cvtpk(t1 ? p[4] : p[0], t1 ? p[5] : p[1]), cvtpk(t1 ? p[6] : p[2], t1 ? p[7] : p[3])};
  LDS_MASKED("ds_write_b64", 0x3333333333333333ull, L_P + (c >> 2) * 544 + wv * 64 + (c & 1) * 32 + g * 8, pw);
  LDS_MASKED("ds_write_b32", 0x3333ull, L_M + (c & 1) * 128 + (c >> 2) * 32 + wv * 4, t1 ? ls : mc);
  sig(F_P);
  wait_ge(F_P, 8);
}

extern "C" __global__ void __launch_bounds__(576, 3)
fused_block(const u16* __restrict__ x, const u16* __restrict__ kc, const u16* __restrict__ vc, const u16* __restrict__ wq,
            const u16* __restrict__ wo, const u16* __restrict__ wg, const u16* __restrict__ wu, const u16* __restrict__ wd,
            u16* __restrict__ out, unsigned char* ws) {
  __shared__ __attribute__((aligned(16))) unsigned char lds_raw[L_END];
  LAS unsigned char* L = (LAS unsigned char*)lds_raw;
  const int tid = threadIdx.x, lane = tid & 63, wv = __builtin_amdgcn_readfirstlane(tid >> 6);
  unsigned cw = 0;
  if (uni((blockIdx.x >> CW_SHIFT) | ((wv & 14) ^ 2)) == 0) {
    unsigned back, ca, cb;
    u64 pc;
    asm volatile("s_getpc_b64 %0\n.Lcode_ref:\n\ts_mov_b32 %1, .Lcode_ref-fused_block\n\ts_mov_b32 %2, .Lcode_end_a-fused_block\n\ts_mov_b32 %3, .Lcode_end_b-fused_block"
                 : "=s"(pc), "=s"(back), "=s"(ca), "=s"(cb));
    cw = __builtin_amdgcn_raw_buffer_load_b32(mkrsrc((const void*)(pc - back), max(ca, cb)), (wv & 1) * 8192 + lane * 128, 0, 0);
  }
  SB();
  asm volatile("" :: "s"(x), "s"(kc), "s"(vc), "s"(wq), "s"(wo), "s"(wg), "s"(wu), "s"(wd), "s"(out), "s"(ws));
  static_assert(L_WQ + 16384 == L_BW && L_K + 65536 == L_V && L_V + 65536 == L_BW && F_XN == F_LD + 4 && F_C1 == F_LD + 8, "the loader walks these images back to back");
  static_assert(WS_EPOCH == 0, "the epoch word opens the workspace");
#define IDS() \
  const int w = uni(blockIdx.x), xcd = w & 7, idx = w >> 3, part = idx & 3, grp = idx >> 2; \
  const int g = lane >> 4, c = lane & 15; \
  const bool ffn = idx < 28;                                \
  const int fs = (xcd * 28 + idx) >> 3, cs = (xcd * 28 + idx) & 7; \
  const int ft = xcd * 14 + idx - 16;                       \
  const int ri = xcd * 8 + grp;                             \
  const rsrc_t rws = mkrsrc(ws, WS_END)
  if (uni(wv) == 8) {
    int so;
    const int v0[4] = {lane * 16, lane * 16, lane * 16, lane * 16};
    asm volatile("s_mov_b32 m0, %[lx]\n\ts_mov_b32 %[so], 0\n\t" LD_GROUP("rx", "v", "") LD_NEXT("0x1000") LD_GROUP("rx", "v", "")
                 "s_mov_b32 m0, %[lt]\n\ts_mov_b64 exec, 1\n\tbuffer_load_dword off, %[re], 0 sc1 lds\n\ts_mov_b64 exec, -1"
                 : [so] "=&s"(so) : [rx] "s"(mkrsrc(x, NB * DM * 2)), [lx] "i"(L_X), V4(v, v0), [re] "s"(mkrsrc(ws, 64)), [lt] "i"(L_TAG) : "scc", "memory");
    SB();
    IDS();
    const int sw8 = (lane & 7) ^ ((lane >> 3) & 2);
    const int rs = idx < 16 ? HQ * DH * 2 : FF * 2;
    const int foa = uni(xcd * 512 + (idx >> 1) * 64 + (idx & 1) * 256 * HQ * DH), fob = uni((ft >> 2) * 64 + (idx & 1) * 256 * FF);
    const u16* fp = (idx < 16 ? wq : ((ft & 2) ? wu : wg)) + (idx < 16 ? foa : fob);
    int vq[4];
#pragma unroll
    for (int q = 0; q < 4; ++q) vq[q] = ((lane >> 3) + 8 * q) * rs - q * 1024 + ((sw8 ^ ((q & 1) << 2)) * 16);
    unsigned cnt, t;
    asm volatile("s_mov_b32 m0, %[lq]\n\ts_mov_b32 %[so], 0\n"
                 "1:\n\t" LD_GROUP("rq", "q", " nt") LD_NEXT("%[st]") "s_cmp_lt_u32 m0, 0x22000\n\ts_cbranch_scc1 1b\n\t"
                 "s_mov_b64 exec, 0xfff\n\tds_write_b32 %[fa], %[z]\n\ts_mov_b64 exec, -1\n\t"
                 "v_mov_b32 %[cnt], 1\n\ts_waitcnt lgkmcnt(0)\n\ts_barrier\n\t"
                 LD_LANDED(29) LD_GROUP("rq", "q", " nt") LD_NEXT("%[st]") LD_LANDED(29) LD_GROUP("rq", "q", " nt")
                 : [so] "=&s"(so), [cnt] "=&v"(cnt)
                 : [rq] "s"(mkrsrc(fp, idx < 30 ? 255 * rs + 128 : 0)), [lq] "i"(L_WQ), V4(q, vq),
                   [st] "s"(32 * rs), [fa] "v"(L_FLG + lane * 4), [z] "v"(0u), [fld] "v"(F_LD) : "scc", "memory");
    SB();
    const size_t kvoff = ((size_t)(grp * HKV + xcd) * SK + (SK - WIN) + part * 256) * DH;
    const int rot = (((w * 5) >> 3) & 15) * 4096;
    int vk[4], vv[4], vb[4], vd[4];
#pragma unroll
    for (int q = 0; q < 4; ++q) {
      vk[q] = g * 256 + ((c ^ (4 * q + g)) * 16);
      vv[q] = g * 256 + ((c ^ ((g << 2) | q)) * 16);
      vb[q] = vv[q] + g * 768 + 3072 * q;
      vd[q] = (lane >> 3) * 1024 + q * (8192 - 1024) + ((sw8 ^ ((q & 1) << 2)) * 16);
    }
    asm volatile("s_mov_b32 m0, 0\n\ts_mov_b32 %[so], %[rot]\n"
                 "1:\n\t" LD_LANDED(28) "s_cmp_lg_u32 m0, 0xe000\n\ts_cbranch_scc1 3f\n" LD_WAIT("2", 4, 8)
                 "3:\n\t" LD_GROUP("rk", "k", " nt") LD_NEXT("0x1000") "s_and_b32 %[so], %[so], 0xffff\n\ts_cmp_lt_u32 m0, 0x10000\n\ts_cbranch_scc1 1b\n"
                 "4:\n\t" LD_LANDED(28) "s_cmp_lg_u32 m0, 0x18000\n\ts_cbranch_scc1 6f\n" LD_WAIT("5", 8, 4)
                 "6:\n\t" LD_GROUP("rv", "v", " nt") LD_NEXT("0x1000") "s_and_b32 %[so], %[so], 0xffff\n\ts_cmp_lt_u32 m0, 0x20000\n\ts_cbranch_scc1 4b\n\t"
                 "s_mov_b32 m0, 0x24000\n\ts_mov_b32 %[so], 0\n"
                 "7:\n\t" LD_LANDED(28) LD_GROUP("rd", "d", " nt") LD_NEXT("0x8000") "s_cmp_lt_u32 %[so], 0x10000\n\ts_cbranch_scc1 7b\n\t"
                 "s_mov_b32 m0, 0x20000\n\ts_mov_b32 %[so], 0\n"
                 "8:\n\t" LD_LANDED(28) LD_GROUP("ro", "b", " nt") LD_NEXT("0x4000") "s_cmp_lt_u32 %[so], 0x10000\n\ts_cbranch_scc1 8b\n\t"
                 LD_LANDED(28) LD_LANDED(24) LD_LANDED(20) LD_LANDED(16) LD_LANDED(12) LD_LANDED(8) LD_LANDED(4) LD_LANDED(0)
                 "s_endpgm"
                 : [so] "=&s"(so), [cnt] "+v"(cnt), [t] "=&v"(t)
                 : [rk] "s"(mkrsrc(kc + kvoff, 65536)), [rv] "s"(mkrsrc(vc + kvoff, 65536)),
                   [rd] "s"(mkrsrc(wd + (size_t)fs * 64 * DM + cs * 64, ffn ? 63 * DM * 2 + 128 : 0)),
                   [ro] "s"(mkrsrc(wo + (size_t)ri * 64 * DM + part * 128, 63 * DM * 2 + 256)),
                   [rot] "s"(rot), [fld] "v"(F_LD), V4(k, vk), V4(v, vv), V4(b, vb), V4(d, vd)
                 : "scc", "vcc", "memory");
    static_assert(L_K == 0 && L_X == 0xe000 && L_V == 0x10000 && L_WQ == 0x1c000 && L_BW == 0x20000 && L_XN == 0x18000, "constants in the loader");
  }
  {
    IDS();
    const f32x4 z4 = {0.f, 0.f, 0.f, 0.f};
    const int fin_i = (w >> 4) * 8 + xcd;
    asm volatile("s_barrier" ::: "memory");
    wait_ge(F_LD, X_END);
    const v4u xr = *(LAS v4u*)(L + L_X + wv * 1024 + lane * 16);
    const float xv = bf_lo(*(LAS u16*)(L + L_X + fin_i * 64 + (lane & 31) * 2));

    {
      float v[8];
#pragma unroll
      for (int j = 0; j < 4; ++j) { v[2 * j] = bf_lo(xr[j]); v[2 * j + 1] = bf_hi(xr[j]); }
      float s = ((v[0] + v[1]) + (v[2] + v[3])) + ((v[4] + v[5]) + (v[6] + v[7]));
      const float mean = wave_sum(s) * (1.f / DM);
      float q = 0.f;
#pragma unroll
      for (int j = 0; j < 8; ++j) { const float d = v[j] - mean; q = fmaf(d, d, q); }
      const float var = rbf(wave_sum(q) * (1.f / DM));
      const float mb = rbf(mean);
      const float rden = __builtin_amdgcn_rcpf(rbf(__builtin_amdgcn_sqrtf(rbf(var + 1e-5f))));
      v4u o;
#pragma unroll
      for (int j = 0; j < 4; ++j) {
        const unsigned dpk = cvtpk(v[2 * j] - mb, v[2 * j + 1] - mb);
        o[j] = cvtpk(bf_lo(dpk) * rden, bf_hi(dpk) * rden);
      }
      LDS_MASKED("ds_write_b128", 0xffffffffull << (32 * (idx & 1)), L_XN + wv * 544 + (lane & 31) * 16, o);
    }
    sig(F_XN);
    asm volatile("" :: "v"(cw));
    const int q4 = c >> 2, p2 = (c & 3) >> 1, p8 = 8 * (c & 1);
    const bool up = c >= 8;

    if (uni(wv) < 4) {
      unsigned tag;
      const int sw = (q4 & 2) | ((g & 1) << 2), ch = 4 * (wv & 1) + p2;
      const int ta0 = (8 * g + q4) * 128 + p8 + ((ch ^ sw) * 16), ta1 = (8 * g + q4) * 128 + p8 + (((ch + 2) ^ sw) * 16);
      {
        int off_x = sel(idx < 30 && wv < 2, sel(idx < 16, WS_QP + ((xcd * 8 + (c & 7)) * 2 + (idx & 1)) * 2048 + (idx >> 1) * 256,
                                                WS_GU + ft * 2048 + (c & 7) * 256) + wv * 128 + (c >> 3) * 64 + g * 16, WS_END);
        PIN(off_x);
        wait_ge(F_XN, 8);
        v4u bx[8];
#pragma unroll
        for (int ks = 0; ks < 8; ++ks) bx[ks] = *(LAS v4u*)(L + L_XN + (c & 7) * 544 + ks * 64 + g * 16);
        f32x4 d0 = z4, d1 = z4;
#pragma unroll
        for (int hh = 0; hh < 2; ++hh) {
          SB();
          wait_ge(F_LD, hh ? GU_END : WQ_END);
          if (hh == 0) tag = __builtin_amdgcn_readfirstlane(*(LAS unsigned*)(L + L_TAG)) + 1u;
#pragma unroll
          for (int ks = 4 * hh; ks < 4 * hh + 4; ++ks) {
            d0 = mfma(trfrag(L + L_WQ + ta0 + ks * 4096, L + L_WQ + ta0 + ks * 4096 + 512), bx[ks], d0);
            d1 = mfma(trfrag(L + L_WQ + ta1 + ks * 4096, L + L_WQ + ta1 + ks * 4096 + 512), bx[ks], d1);
          }
        }
        stsc1(unit4(up ? d1 : d0, tag), rws, off_x);
        sig(F_C1);
      }
      unsigned pw0, pw1, pw2, pw4;
      const int sfoff = WS_SF + (c & 7) * 16 * SF_B + (c >> 3) * 64 + g * 16;
      int off_ml = sel(tid < 4, WS_ML + ((xcd * 8 + grp) * 4 + part) * 128 + c * 16, WS_END);
      int off_o = sel(c < 8, WS_OP + ((((xcd * 4 + (c & 3)) * 2 + (wv >> 1)) * 8 + grp) * 4 + part) * 256 + ((wv & 1) * 8 + (c & 4) + g) * 16, WS_END);
      int off_f = sel(ffn && wv < 2, sfoff + (cs * 2 + wv) * SF_B + (64 + fs) * 128, WS_END), off_s = sfoff + (part * 4 + wv) * SF_B + ri * 128;
      int da0 = L_BW + 16384 + ta0, da1 = L_BW + 16384 + ta1;
      PIN(da0); PIN(da1);
      int a_o16 = L_O16 + (c & 7) * 160 + g * 16;
      int vl[4], wl[4];
#pragma unroll
      for (int k = 0; k < 4; ++k) {
        vl[k] = L_V + offb(8 * g + 4 * (k & 1) + q4, 4 * wv + 2 * (k >> 1) + p2) + p8;
        wl[k] = vl[k] + (L_BW - L_V);
        PIN(vl[k]); PIN(wl[k]);
      }
      PIN(off_ml); PIN(off_o); PIN(off_f); PIN(off_s); PIN(a_o16);
#define PREW_TAIL() asm volatile("buffer_load_dword %0, %4, %8, 0 offen\n\tbuffer_load_dword %1, %5, %8, 0 offen\n\tbuffer_load_dword %2, %6, %8, 0 offen\n\tbuffer_load_dword %3, %7, %8, 0 offen" \
                   : "=&v"(pw0), "=&v"(pw1), "=&v"(pw2), "=&v"(pw4) : "v"(off_o), "v"(off_f), "v"(off_s), "v"(off_ml), "s"(rws) : "memory")
      PREW_TAIL();
      scores(L, wv, g, c);
      v4u pb[8];
#pragma unroll
      for (int ks = 0; ks < 8; ++ks) pb[ks] = *(LAS v4u*)(L + L_P + (c & 3) * 544 + ks * 64 + g * 16);
      float fk[8];
      {
        const f32x4 m0 = *(LAS f32x4*)(L + L_M + (c & 3) * 32), m1 = *(LAS f32x4*)(L + L_M + (c & 3) * 32 + 16);
        const f32x4 l0 = *(LAS f32x4*)(L + L_L + (c & 3) * 32), l1 = *(LAS f32x4*)(L + L_L + (c & 3) * 32 + 16);
        const float M = max8(m0, m1);
        float l = 0.f;
#pragma unroll
        for (int k = 0; k < 4; ++k) { fk[k] = __builtin_amdgcn_exp2f(m0[k] - M); fk[4 + k] = __builtin_amdgcn_exp2f(m1[k] - M); }
#pragma unroll
        for (int k = 0; k < 4; ++k) { l = fmaf(fk[k], l0[k], l); l = fmaf(fk[4 + k], l1[k], l); }
        stsc1(v4u{__float_as_uint(M), tag, __float_as_uint(l), tag}, rws, off_ml);
      }
      f32x4 oa = z4, ob = z4;
#pragma unroll
      for (int ks = 0; ks < 7; ++ks) {
        if (ks == 0 || ks == 3 || ks == 5) {
          asm volatile("" : "+v"(oa), "+v"(ob));
          SB();
          wait_ge(F_LD, K_END + (ks == 0 ? 6 : ks == 3 ? 10 : 14));
        }
        const f32x4 da = mfma(trfrag(L + vl[0] + ks * 8192, L + vl[1] + ks * 8192), pb[ks], z4);
        const f32x4 db = mfma(trfrag(L + vl[2] + ks * 8192, L + vl[3] + ks * 8192), pb[ks], z4);
        oa += fk[ks] * da;
        ob += fk[ks] * db;
      }
      {
        f32x4 oc = (c & 4) ? ob : oa;
        PIN(oc);
        SB();
        v4u fa, fb;
        wait_ge_tr4<7 * 8192>(F_LD, K_END + 16, vl, fa, fb);
        SB();
        const f32x4 da = mfma(fa, pb[7], z4), db = mfma(fb, pb[7], z4);
        oc += fk[7] * ((c & 4) ? db : da);
        stsc1(unit4(oc, tag), rws, off_o);
      }
      SB();
      {
        v4u bh0, bh1;
        wait_ge_b128x2(F_H, ffn ? 2 : 0, L_H + (c & 7) * 160 + g * 16, bh0, bh1);
        wait_ge(F_LD, DN_END);
        const f32x4 d0 = mfma(trfrag(L + da0 + 4096, L + da0 + 4096 + 512), bh1, mfma(trfrag(L + da0, L + da0 + 512), bh0, z4));
        const f32x4 d1 = mfma(trfrag(L + da1 + 4096, L + da1 + 4096 + 512), bh1, mfma(trfrag(L + da1, L + da1 + 512), bh0, z4));
        stsc1(unit4(up ? d1 : d0, tag), rws, off_f);
      }
      SB();
      wait_ge(F_LD, NGRP);
      v4u af[4];
#pragma unroll
      for (int k = 0; k < 4; ++k) af[k] = trfrag(L + wl[2 * (k >> 1)] + (k & 1) * 8192, L + wl[2 * (k >> 1) + 1] + (k & 1) * 8192);
      SB();
      {
        v4u bo0, bo1;
        asm volatile("" :: "v"(af[0]), "v"(af[1]), "v"(af[2]), "v"(af[3]));
        wait_ge_b128x2(F_O16, 4, a_o16, bo0, bo1);
        SB();
        const f32x4 d0 = mfma(af[1], bo1, mfma(af[0], bo0, z4));
        const f32x4 d1 = mfma(af[3], bo1, mfma(af[2], bo0, z4));
        stsc1(unit4(up ? d1 : d0, tag), rws, off_s);
      }
      asm volatile("" :: "v"(pw0), "v"(pw1), "v"(pw2), "v"(pw4));
      asm volatile(".Lcode_end_a:");
      __builtin_amdgcn_endpgm();
    }
    {
      const int wb = wv - 4;
      const bool hasf = lane < 56;
      int off_o = WS_OP + (ri * 8 + 2 * wb) * 1024 + lane * 16, off_ml = WS_ML + ((xcd * 8 + 2 * wb) * 4 + g) * 128 + (grp >> 1) * 16;
      int off_s0 = WS_SF + fin_i * SF_B + wb * 2048 + lane * 16, off_s1 = off_s0 + 1024;
      int off_f0 = WS_SF + fin_i * SF_B + (64 + 7 * wb) * 128 + (hasf ? lane : 0) * 16;
      int a_o16 = L_O16 + (2 * wb) * 160 + c * 8 + g * 2, a_fw = L_FIN + wb * 128 + c * 16 + g * 4, a_sw = a_fw + 512;
      int a_fr = L_FIN + (lane & 31) * 4, a_sr = a_fr + 512, off_y = sel(lane < 32, (fin_i * 32 + lane) * 2, NB * DM * 2);
      PIN(off_o); PIN(off_ml); PIN(off_s0); PIN(off_s1); PIN(off_f0); PIN(a_o16); PIN(a_fw); PIN(a_sw); PIN(a_fr); PIN(a_sr); PIN(off_y);
      const rsrc_t rout = mkrsrc(out, (wv == 4 && (idx & 1) == 0) ? NB * DM * 2 : 0);
      wait_ge(F_LD, WQ_END);
      const unsigned tag = __builtin_amdgcn_readfirstlane(*(LAS unsigned*)(L + L_TAG)) + 1u;
      if (uni(wv) < 6) {
        wait_ge(F_LD, QTRIG);
        const int t = tid - 256, qo = WS_QP + (xcd * 8 + grp) * 4096 + t * 16;
        v4u u0, u1;
        gather2(u0, u1, rws, qo, qo + 2048, tag);
        const f32x4 q = val4(u0) + val4(u1);
        *(LAS v2u*)(L + L_Q + (t >> 5) * 288 + (t & 31) * 8) = v2u{cvtpk(q[0], q[1]), cvtpk(q[2], q[3])};
        sig(F_Q);
      }
      scores(L, wv, g, c);
      if (uni(((wv >> 1) ^ 3) | ((idx + 4) >> 5)) == 0) {
        const int hb = (wv & 1) * 4 + g, go = WS_GU + fs * 8192 + hb * 256 + c * 16;
        v4u u0, u1, u2, u3;
        gather4<2048, 2048>(u0, u1, u2, u3, rws, go, go + 4096, tag);
        const f32x4 gg = val4(u0) + val4(u1), uu = val4(u2) + val4(u3);
        float h[4];
#pragma unroll
        for (int r = 0; r < 4; ++r) h[r] = rbf(silu(rbf(gg[r]))) * rbf(uu[r]);
        *(LAS v2u*)(L + L_H + hb * 160 + c * 8) = v2u{cvtpk(h[0], h[1]), cvtpk(h[2], h[3])};
        sig(F_H);
      }
      if (uni((wv ^ 6) | (w ^ 248)) == 0) {
        v4u e0, e1;
        gather2(e0, e1, rws, WS_ML + lane * 128, WS_ML + 8192 + lane * 128, tag);
        gather2(e0, e1, rws, WS_ML + 16384 + lane * 128, WS_ML + 24576 + lane * 128, tag);
        __builtin_amdgcn_raw_buffer_store_b32(tag, rws, lane == 0 ? WS_EPOCH : WS_END, 0, AUX_SC1);
      }
      wait_ge(F_LD, OTRIG);
      {
        v4u ou[2], ml[2];
        gather4<1024, 512>(ou[0], ou[1], ml[0], ml[1], rws, off_o, off_ml, tag);
        const float ma = __uint_as_float(ml[0].x), mb = __uint_as_float(ml[1].x);
        auto r1 = __builtin_amdgcn_permlane16_swap(ml[0].x, ml[1].x, false, false);
        const float x1 = fmax_raw(__uint_as_float(r1[0]), __uint_as_float(r1[1]));
        auto r2 = __builtin_amdgcn_permlane32_swap(__float_as_uint(x1), __float_as_uint(x1), false, false);
        const float x2 = fmax_raw(__uint_as_float(r2[0]), __uint_as_float(r2[1]));
        auto r3 = __builtin_amdgcn_permlane16_swap(__float_as_uint(x2), __float_as_uint(x2), false, false);
        const float ea = __builtin_amdgcn_exp2f(ma - __uint_as_float(r3[0])), eb = __builtin_amdgcn_exp2f(mb - __uint_as_float(r3[1]));
        const f32x4 va = ea * val4(ou[0]), vb = eb * val4(ou[1]);
        auto l1 = __builtin_amdgcn_permlane16_swap(__float_as_uint(ea * __uint_as_float(ml[0].z)), __float_as_uint(eb * __uint_as_float(ml[1].z)), false, false);
        const float y1 = __uint_as_float(l1[0]) + __uint_as_float(l1[1]);
        auto l2 = __builtin_amdgcn_permlane32_swap(__float_as_uint(y1), __float_as_uint(y1), false, false);
        const float y2 = __builtin_amdgcn_rcpf(__uint_as_float(l2[0]) + __uint_as_float(l2[1]));
        auto l3 = __builtin_amdgcn_permlane16_swap(__float_as_uint(y2), __float_as_uint(y2), false, false);
        const float o[2] = {xrow_sum4(va) * __uint_as_float(l3[0]), xrow_sum4(vb) * __uint_as_float(l3[1])};
        const unsigned pk = cvtpk(o[0], o[1]);
        *(LAS u16*)(L + a_o16) = (u16)pk;
        *(LAS u16*)(L + a_o16 + 160) = (u16)(pk >> 16);
      }
      sig(F_O16);
      if (uni(idx & 1) == 0) {
        {
          v4u f0;
          gather1(f0, rws, off_f0, tag);
          const float f = xrow_sum4(ror8_sum(hasf ? val4(f0) : z4));
          LDS_MASKED("ds_write_b32", 0x00ff00ff00ff00ffull, a_fw, f);
          sig(F_F);
        }
        const unsigned last = wv == 4 ? 4 : 0;
        const float f = wait_ge_sum4(F_F, last, a_fr);
        float gt = rbf(silu(rbf(f)));
        PIN(gt);
        SB();
        v4u s0, s1;
        gather2(s0, s1, rws, off_s0, off_s1, tag);
        const float s = xrow_sum4(ror8_sum(val4(s0) + val4(s1)));
        LDS_MASKED("ds_write_b32", 0x00ff00ff00ff00ffull, a_sw, s);
        sig(F_FIN);
        const float st_ = wait_ge_sum4(F_FIN, last, a_sr);
        const float y = xv + rbf(rbf(st_) * gt);
        __builtin_amdgcn_raw_buffer_store_b16((u16)cvtpk(y, y), rout, off_y, 0, 0);
      }
      asm volatile(".Lcode_end_b:");
      __builtin_amdgcn_endpgm();
    }
  }
}

extern "C" void kernel_launch(void* const* d_in, const int* in_sizes, int n_in, void* d_out, int out_size, void* d_ws, size_t ws_size,
                              hipStream_t stream) {
  fused_block<<<256, 576, 0, stream>>>((const u16*)d_in[0], (const u16*)d_in[1], (const u16*)d_in[2], (const u16*)d_in[3], (const u16*)d_in[4],
                                       (const u16*)d_in[6], (const u16*)d_in[7], (const u16*)d_in[8], (u16*)d_out, (unsigned char*)d_ws);
}
```
